```python
import torch

def _softmax(x, axis=-1):
    return torch.softmax(x, dim=axis)
def _gelu(x):
    return torch.nn.functional.gelu(x, approximate="tanh")
def _silu(x):
    return torch.nn.functional.silu(x)
def _sigmoid(x):
    return torch.sigmoid(x)
def _layernorm(x, axis=-1, eps=1e-5):
    m = x.mean(dim=axis, keepdim=True)
    v = x.var(dim=axis, keepdim=True, unbiased=False)
    return (x - m) / torch.sqrt(v + eps)
def _scatter_add(tgt, idx, src, axis=0):
    return tgt.clone().index_add_(axis, idx, src)
def _segment_sum(src, seg, n):
    out = torch.zeros((n,) + src.shape[1:], dtype=src.dtype)
    return out.index_add_(0, seg, src)
def _pad(x, pw):
    flat = tuple(v for pair in reversed(pw) for v in pair)
    return torch.nn.functional.pad(x, flat)

kernel_name = 'standalone__deep_cross_wide_deep__recsys'

def setup_inputs():

    _w = (lambda a: (lambda t: t.to(torch.bfloat16) if t.is_floating_point() else t)(torch.as_tensor(a)))
    B, F, D = 16384, 100, 32
    V, Nd = min(1000, 100000), 13
    Din = F * D
    H = 4 * D
    return {
        "idx": _w((lambda lo, hi, *s: torch.randint(lo, hi, s))(0, V, B, F).to(torch.int64)),
        "table": _w((lambda *s: torch.randn(*s, dtype=torch.bfloat16) * 1.6)(V, D) * 0.05),
        "x_wide": _w((lambda *s: torch.randn(*s, dtype=torch.bfloat16) * 1.6)(B, Nd)),
        "w_wide": _w((lambda *s: torch.randn(*s, dtype=torch.bfloat16) * 1.6)(Nd) * 0.05),
        "b_wide": _w((lambda *s: torch.randn(*s, dtype=torch.bfloat16) * 1.6)(1) * 0.01),
        "W1": _w((lambda *s: torch.randn(*s, dtype=torch.bfloat16) * 1.6)(Din, H) * (2.0 / Din) ** 0.5),
        "b1": _w((lambda *s: torch.randn(*s, dtype=torch.bfloat16) * 1.6)(H) * 0.01),
        "W2": _w((lambda *s: torch.randn(*s, dtype=torch.bfloat16) * 1.6)(H) * (1.0 / H) ** 0.5),
        "b2": _w((lambda *s: torch.randn(*s, dtype=torch.bfloat16) * 1.6)(1) * 0.01),
    }

def reference(idx, table, x_wide, w_wide, b_wide, W1, b1, W2, b2):
    emb = table[idx]
    deep_in = emb.reshape(emb.shape[0], -1)
    h = deep_in @ W1 + b1
    h = h * (lambda x, dt: x.to(dt))(h > 0, h.dtype)
    deep_logit = h @ W2 + b2
    wide_logit = x_wide @ w_wide + b_wide
    return _sigmoid(wide_logit + deep_logit)
```

```cpp
#include <hip/hip_runtime.h>
#include <hip/hip_bf16.h>
#include <stdint.h>

typedef unsigned int u32;
typedef unsigned long long u64;
typedef __attribute__((ext_vector_type(4))) unsigned u32x4;
typedef __attribute__((ext_vector_type(4))) int i32x4;
typedef __attribute__((ext_vector_type(4))) float f32x4;
typedef __attribute__((ext_vector_type(16))) float f32x16;
typedef __attribute__((ext_vector_type(8))) __bf16 bf16x8;
#define LAS __attribute__((address_space(3)))

constexpr int NB = 16384, NF = 100, NV = 1000, ND = 13, NH = 128;
constexpr int PLANE = 16032;
constexpr int RED_OFF = 4 * PLANE;
constexpr int LDS_BYTES = RED_OFF + 98304;
#define WDEPTH 6
constexpr int WRING = 8;
constexpr int NSTEP = 50;

__device__ __forceinline__ float bf2f(unsigned short x) { return __uint_as_float((u32)x << 16); }
__device__ __forceinline__ float rbf(float x) {
  return __bfloat162float(__float2bfloat16(x));
}

#define STAMP(i)

__global__ __launch_bounds__(256, 1) void wd_main(const int* __restrict__ idx, const char* __restrict__ table,
                                                  const char* __restrict__ W1, const char* __restrict__ b1,
                                                  const char* __restrict__ W2, float* __restrict__ part
) {
  extern __shared__ __attribute__((aligned(16))) char smem[];
  LAS char* lds = (LAS char*)smem;
  STAMP(0);
  const int tid = threadIdx.x;
  const int lane = tid & 63;
  const int w = __builtin_amdgcn_readfirstlane(tid >> 6);
  const int i32 = lane & 31, h = lane >> 5;
  const int bid = blockIdx.x;
  const int xcd = bid & 7, j = bid >> 3;
  const int half = j & 1;
  const int pj = j >> 1;
  const int pair = pj * 8 + xcd;
  const int b0 = pair * 128;
  const int ph = pj % 12;
  const int rot3 = ph % 3, r4 = ph / 3;

  u32x4 tb[16];
#pragma unroll
  for (int i = 0; i < 16; ++i) {
    if (i < 15 || tid < 160) tb[i] = *(const u32x4*)(table + i * 4096 + tid * 16);
  }
  const __amdgpu_buffer_rsrc_t rW = __builtin_amdgcn_make_buffer_rsrc((void*)W1, 0, 3200 * 256, 0x00020000);
  int ixe[4];
  i32x4 ix[3][4];
#pragma unroll
  for (int t = 0; t < 4; ++t) {
    const int row = b0 + 32 * ((w + t) & 3) + i32;
    ixe[t] = idx[row * NF + 96 + w];
  }
  int xo[3];
#pragma unroll
  for (int x = 0; x < 3; ++x) { int X = x + rot3; xo[x] = X >= 3 ? X - 3 : X; }
#pragma unroll
  for (int t = 0; t < 4; ++t) {
    const int row = b0 + 32 * ((w + t) & 3) + i32;
    ix[0][t] = *(const i32x4*)(idx + row * NF + 24 * w + 12 * h + 4 * xo[0]);
  }
  const u32 voffW = (u32)(32 * (24 * w + 12 * h)) * 256u + half * 128 + 4 * i32;
  u32 voffE[2];
#pragma unroll
  for (int e = 0; e < 2; ++e) voffE[e] = (u32)(32 * (96 + w) + 8 * ((2 * e + h + r4) & 3)) * 256u + half * 128 + 4 * i32;
  u32 raw[WRING][8];
  auto loadW = [&](int s) __attribute__((always_inline)) {
    if (s < 2) {
#pragma unroll
      for (int jj = 0; jj < 8; ++jj) raw[s % WRING][jj] = __builtin_amdgcn_raw_buffer_load_b32(rW, voffE[s] + jj * 256, 0, 0);
    } else {
      const int ss = s - 2, x = ss >> 4, m = (ss >> 2) & 3, p = ss & 3;
      const int so = xo[x] * 32768 + ((m + r4) & 3) * 2048 + p * 8192;
#pragma unroll
      for (int jj = 0; jj < 8; ++jj) raw[s % WRING][jj] = __builtin_amdgcn_raw_buffer_load_b32(rW, voffW + jj * 256, so, 0);
    }
  };
#pragma unroll
  for (int s = 0; s < WDEPTH; ++s) loadW(s);
#pragma unroll
  for (int x = 1; x < 3; ++x)
#pragma unroll
    for (int t = 0; t < 4; ++t) {
      const int row = b0 + 32 * ((w + t) & 3) + i32;
      ix[x][t] = *(const i32x4*)(idx + row * NF + 24 * w + 12 * h + 4 * xo[x]);
    }
  __builtin_amdgcn_sched_barrier(0);
  {
    const u32 ta = (u32)(((tid & 3) - r4) & 3) * PLANE + (u32)(tid >> 2) * 16;
#pragma unroll
    for (int i = 0; i < 16; ++i) {
      if (i < 15 || tid < 160) *(LAS u32x4*)(lds + ta + i * 1024) = tb[i];
    }
  }
  STAMP(1);
  __builtin_amdgcn_s_waitcnt(0xC07F);
  __builtin_amdgcn_s_barrier();
  STAMP(2);
  __builtin_amdgcn_sched_barrier(0);

  f32x16 acc[4][2];
#pragma unroll
  for (int t = 0; t < 4; ++t)
#pragma unroll
    for (int n = 0; n < 2; ++n)
#pragma unroll
      for (int r = 0; r < 16; ++r) acc[t][n][r] = 0.f;

  u32x4 af[3][4];
  u32x4 wf[2][2];
  auto loadA = [&](int s) __attribute__((always_inline)) {
    if (s < 2) {
#pragma unroll
      for (int t = 0; t < 4; ++t) af[s % 3][t] = *(LAS u32x4*)(lds + (u32)ixe[t] * 16 + h * PLANE + s * 2 * PLANE);
    } else {
      const int ss = s - 2, x = ss >> 4, m = (ss >> 2) & 3, p = ss & 3;
#pragma unroll
      for (int t = 0; t < 4; ++t) af[s % 3][t] = *(LAS u32x4*)(lds + (u32)ix[x][t][p] * 16 + m * PLANE);
    }
  };
  auto permW = [&](int s) __attribute__((always_inline)) {
#pragma unroll
    for (int d = 0; d < 4; ++d) {
      wf[s & 1][0][d] = __builtin_amdgcn_perm(raw[s % WRING][2 * d + 1], raw[s % WRING][2 * d], 0x05040100);
      wf[s & 1][1][d] = __builtin_amdgcn_perm(raw[s % WRING][2 * d + 1], raw[s % WRING][2 * d], 0x07060302);
    }
  };
  loadA(0); loadA(1); permW(0);
  __builtin_amdgcn_sched_barrier(0);
#pragma unroll
  for (int s = 0; s < NSTEP; ++s) {
    if (s + WDEPTH < NSTEP) loadW(s + WDEPTH);
    if (s + 2 < NSTEP) loadA(s + 2);
    if (s + 1 < NSTEP) permW(s + 1);
#pragma unroll
    for (int t = 0; t < 4; ++t)
#pragma unroll
      for (int n = 0; n < 2; ++n)
        acc[t][n] = __builtin_amdgcn_mfma_f32_32x32x16_bf16(__builtin_bit_cast(bf16x8, wf[s & 1][n]), __builtin_bit_cast(bf16x8, af[s % 3][t]), acc[t][n], 0, 0, 0);
#pragma unroll
    for (int q = 0; q < 8; ++q) {
      __builtin_amdgcn_sched_group_barrier(0x8, 1, 0);
      __builtin_amdgcn_sched_group_barrier(0x20, 1, 0);
      if (q < 4) __builtin_amdgcn_sched_group_barrier(0x100, 1, 0);
      __builtin_amdgcn_sched_group_barrier(0x2, 2, 0);
    }
    __builtin_amdgcn_sched_barrier(0);
  }
  STAMP(3);

#pragma unroll
  for (int t = 1; t < 4; ++t) {
    const int d = (w + t) & 3;
    LAS char* dst = lds + RED_OFF + (d * 3 + (t - 1)) * 8192 + lane * 16;
#pragma unroll
    for (int n = 0; n < 2; ++n)
#pragma unroll
      for (int u = 0; u < 4; ++u) {
        f32x4 v = {acc[t][n][4 * u], acc[t][n][4 * u + 1], acc[t][n][4 * u + 2], acc[t][n][4 * u + 3]};
        *(LAS f32x4*)(dst + (n * 4 + u) * 1024) = v;
      }
  }
  u32x4 b1v[4], w2v[4];
#pragma unroll
  for (int u = 0; u < 4; ++u) {
    b1v[u] = *(const u32x4*)(b1 + (half * 64 + 16 * u + 8 * h) * 2);
    w2v[u] = *(const u32x4*)(W2 + (half * 64 + 16 * u + 8 * h) * 2);
  }
  __builtin_amdgcn_s_waitcnt(0xC07F);
  __builtin_amdgcn_s_barrier();
  STAMP(4);
  float psum = 0.f;
  {
    LAS char* src = lds + RED_OFF + (w * 3) * 8192 + lane * 16;
#pragma unroll
    for (int n = 0; n < 2; ++n)
#pragma unroll
      for (int u = 0; u < 4; ++u) {
        f32x4 v0 = *(LAS f32x4*)(src + (n * 4 + u) * 1024);
        f32x4 v1 = *(LAS f32x4*)(src + 8192 + (n * 4 + u) * 1024);
        f32x4 v2 = *(LAS f32x4*)(src + 16384 + (n * 4 + u) * 1024);
#pragma unroll
        for (int r = 0; r < 4; ++r) {
          float a = (acc[0][n][4 * u + r] + v0[r]) + (v1[r] + v2[r]);
          const u32 bw = b1v[u][r], ww = w2v[u][r];
          const float bb = __uint_as_float(n ? (bw & 0xffff0000u) : (bw << 16));
          const float wv = __uint_as_float(n ? (ww & 0xffff0000u) : (ww << 16));
          float hv = rbf(rbf(a) + bb);
          hv = hv > 0.f ? hv : 0.f;
          psum = fmaf(hv, wv, psum);
        }
      }
  }
  psum += __shfl_xor(psum, 32);
  if (h == 0) part[half * NB + b0 + 32 * w + i32] = psum;
  STAMP(5);
}

__global__ __launch_bounds__(256) void wd_final(const float* __restrict__ part, const unsigned short* __restrict__ xw,
                                                const unsigned short* __restrict__ ww, const unsigned short* __restrict__ bw,
                                                const unsigned short* __restrict__ b2, __hip_bfloat16* __restrict__ out) {
  const int b = blockIdx.x * 256 + threadIdx.x;
  float deep = rbf(rbf(part[b] + part[NB + b]) + bf2f(b2[0]));
  float wide = 0.f;
#pragma unroll
  for (int k = 0; k < ND; ++k) wide = fmaf(bf2f(xw[b * ND + k]), bf2f(ww[k]), wide);
  wide = rbf(rbf(wide) + bf2f(bw[0]));
  float z = rbf(wide + deep);
  out[b] = __float2bfloat16(1.f / (1.f + __expf(-z)));
}

extern "C" void kernel_launch(void* const* d_in, const int* in_sizes, int n_in, void* d_out, int out_size, void* d_ws,
                              size_t ws_size, hipStream_t stream) {
  const int* idx = (const int*)d_in[0];
  const char* table = (const char*)d_in[1];
  const unsigned short* xw = (const unsigned short*)d_in[2];
  const unsigned short* ww = (const unsigned short*)d_in[3];
  const unsigned short* bw = (const unsigned short*)d_in[4];
  const char* W1 = (const char*)d_in[5];
  const char* b1 = (const char*)d_in[6];
  const char* W2 = (const char*)d_in[7];
  const unsigned short* b2 = (const unsigned short*)d_in[8];
  float* part = (float*)d_ws;
  (void)hipFuncSetAttribute((const void*)wd_main, hipFuncAttributeMaxDynamicSharedMemorySize, LDS_BYTES);
  wd_main<<<256, 256, LDS_BYTES, stream>>>(idx, table, W1, b1, W2, part
  );
  wd_final<<<NB / 256, 256, 0, stream>>>(part, xw, ww, bw, b2, (__hip_bfloat16*)d_out);
}
```

```cpp
#include <hip/hip_runtime.h>
#include <hip/hip_bf16.h>
#include <stdint.h>
#include <type_traits>

typedef unsigned int u32;
typedef unsigned long long u64;
typedef __attribute__((ext_vector_type(4))) unsigned u32x4;
typedef __attribute__((ext_vector_type(2))) unsigned u32x2;
typedef __attribute__((ext_vector_type(4))) float f32x4;
typedef __attribute__((ext_vector_type(8))) __bf16 bf16x8;
#define LAS __attribute__((address_space(3)))

constexpr int NB = 16384, NF = 100, NV = 1000, ND = 13, NH = 128;
constexpr int PLANE = 16416;
constexpr int RED_OFF = 3 * PLANE + 16000;
constexpr int IDX_OFF = RED_OFF + 512;
constexpr int LDS_BYTES = RED_OFF + 98304;
#define WDEPTH 2
constexpr int WRING = WDEPTH + 1;
constexpr int NSTEP = 25;
#define EPI_STEP 20

template <int I, int N, class F>
__device__ __forceinline__ void static_for(F&& f) {
  if constexpr (I < N) { f(std::integral_constant<int, I>{}); static_for<I + 1, N>(f); }
}
__device__ __forceinline__ float bf2f(unsigned short x) { return __uint_as_float((u32)x << 16); }
__device__ __forceinline__ float rbf(float x) { return __bfloat162float(__float2bfloat16(x)); }

#define STAMP(i)

__global__ __launch_bounds__(256, 1) void wd_main(const int* __restrict__ idx, const char* __restrict__ table,
                                                  const unsigned short* __restrict__ xw, const unsigned short* __restrict__ ww,
                                                  const unsigned short* __restrict__ bw, const char* __restrict__ W1,
                                                  const char* __restrict__ b1, const char* __restrict__ W2,
                                                  const unsigned short* __restrict__ b2, __hip_bfloat16* __restrict__ out
) {
  extern __shared__ __attribute__((aligned(16))) char smem[];
  LAS char* lds = (LAS char*)smem;
  STAMP(0);
  const int tid = threadIdx.x;
  const int lane = tid & 63;
  const int w = __builtin_amdgcn_readfirstlane(tid >> 6);
  const int c16 = lane & 15, g = lane >> 4;
  const int bid = blockIdx.x;
  const int j32 = bid >> 3;
  const int b0 = bid * 64;
  const int ph = j32 % 12;
  const int rot3 = ph % 3, r4 = ph / 3;

  const __amdgpu_buffer_rsrc_t rI = __builtin_amdgcn_make_buffer_rsrc((void*)(idx + (size_t)b0 * NF), 0, 64 * NF * 4, 0x00020000);
  const __amdgpu_buffer_rsrc_t rT = __builtin_amdgcn_make_buffer_rsrc((void*)table, 0, NV * 64, 0x00020000);
  const __amdgpu_buffer_rsrc_t rW = __builtin_amdgcn_make_buffer_rsrc((void*)W1, 0, 3200 * 256, 0x00020000);

  u32x4 ib[7];
#pragma unroll
  for (int i = 0; i < 7; ++i) ib[i] = __builtin_amdgcn_raw_buffer_load_b128(rI, tid * 16, i * 4096, 2);
  u32x4 tb[16];
  int tpc[16];
#pragma unroll
  for (int i = 0; i < 16; ++i) {
    tpc[i] = (4 * i + w + 2 * j32) & 63;
    tb[i] = __builtin_amdgcn_raw_buffer_load_b128(rT, lane * 16, tpc[i] * 1024, 0);
  }
  int xo[3];
#pragma unroll
  for (int x = 0; x < 3; ++x) { int X = x + rot3; xo[x] = X >= 3 ? X - 3 : X; }
  const u32 voffW = (u32)(32 * (8 * w + 2 * g)) * 256u + 16 * c16;
  const u32 voffE = (u32)(32 * (96 + w) + 8 * ((g + r4) & 3)) * 256u + 16 * c16;
  u32x4 raw[WRING][8];
  auto loadW = [&](int s) __attribute__((always_inline)) {
    if (s == 24) {
#pragma unroll
      for (int jj = 0; jj < 8; ++jj) raw[s % WRING][jj] = __builtin_amdgcn_raw_buffer_load_b128(rW, voffE + jj * 256, 0, 0);
    } else {
      const int x = s >> 3, m = (s >> 1) & 3, p = s & 1;
      const int so = xo[x] * 262144 + ((m + r4) & 3) * 2048 + p * 8192;
#pragma unroll
      for (int jj = 0; jj < 8; ++jj) raw[s % WRING][jj] = __builtin_amdgcn_raw_buffer_load_b128(rW, voffW + jj * 256, so, 0);
    }
  };
#pragma unroll
  for (int s = 0; s < WDEPTH; ++s) loadW(s);
  __builtin_amdgcn_sched_barrier(0);
#pragma unroll
  for (int i = 0; i < 7; ++i) *(LAS u32x4*)(lds + IDX_OFF + i * 4096 + tid * 16) = ib[i];
  {
    const u32 ta = (u32)(((lane & 3) - r4) & 3) * PLANE + (u32)(lane >> 2) * 16;
#pragma unroll
    for (int i = 0; i < 16; ++i) *(LAS u32x4*)(lds + ta + tpc[i] * 256) = tb[i];
  }
  STAMP(1);
  __builtin_amdgcn_s_waitcnt(0xC07F);
  __builtin_amdgcn_s_barrier();
  STAMP(2);
  __builtin_amdgcn_sched_barrier(0);

  u32 ad[3][4][2], ade[4];
#pragma unroll
  for (int x = 0; x < 3; ++x)
#pragma unroll
    for (int t = 0; t < 4; ++t) {
      const int row = 16 * ((w + t) & 3) + c16;
      u32x2 v = *(LAS u32x2*)(lds + IDX_OFF + row * 400 + (32 * xo[x] + 8 * w + 2 * g) * 4);
      ad[x][t][0] = v[0] * 16; ad[x][t][1] = v[1] * 16;
    }
#pragma unroll
  for (int t = 0; t < 4; ++t) {
    const int row = 16 * ((w + t) & 3) + c16;
    ade[t] = *(LAS u32*)(lds + IDX_OFF + row * 400 + (96 + w) * 4) * 16 + g * PLANE;
  }
  const int myrow = b0 + 16 * w + c16;
  const __amdgpu_buffer_rsrc_t rX = __builtin_amdgcn_make_buffer_rsrc((void*)(xw + (size_t)b0 * ND), 0, 64 * ND * 2, 0x00020000);
  const __amdgpu_buffer_rsrc_t rWW = __builtin_amdgcn_make_buffer_rsrc((void*)ww, 0, ND * 2, 0x00020000);
  unsigned short xv[4], wv4[4];
  u32x4 b1v[4], w2v[4];
  auto loadEpi = [&]() __attribute__((always_inline)) {
#pragma unroll
    for (int q = 0; q < 4; ++q) {
      xv[q] = __builtin_amdgcn_raw_buffer_load_b16(rX, ((16 * w + c16) * ND + (q == 3 ? 12 : g + 4 * q)) * 2, 0, 2);
      wv4[q] = __builtin_amdgcn_raw_buffer_load_b16(rWW, (g + 4 * q) * 2, 0, 0);
    }
#pragma unroll
    for (int r = 0; r < 4; ++r) {
      b1v[r] = *(const u32x4*)(b1 + (32 * g + 8 * r) * 2);
      w2v[r] = *(const u32x4*)(W2 + (32 * g + 8 * r) * 2);
    }
  };
  const float b2f_ = bf2f(b2[0]), bwf_ = bf2f(bw[0]);

  f32x4 acc[4][8];
#pragma unroll
  for (int t = 0; t < 4; ++t)
#pragma unroll
    for (int n = 0; n < 8; ++n) acc[t][n] = f32x4{0.f, 0.f, 0.f, 0.f};

  u32x4 af[2][4];
  auto loadA = [&](int s) __attribute__((always_inline)) {
    if (s == 24) {
#pragma unroll
      for (int t = 0; t < 4; ++t) af[s & 1][t] = *(LAS u32x4*)(lds + ade[t]);
    } else {
      const int x = s >> 3, m = (s >> 1) & 3, p = s & 1;
#pragma unroll
      for (int t = 0; t < 4; ++t) af[s & 1][t] = *(LAS u32x4*)(lds + ad[x][t][p] + m * PLANE);
    }
  };
  u32x4 wf[2][2];
  auto permW = [&](int s, int cc) __attribute__((always_inline)) {
#pragma unroll
    for (int d = 0; d < 4; ++d) {
      wf[cc & 1][0][d] = __builtin_amdgcn_perm(raw[s % WRING][2 * d + 1][cc], raw[s % WRING][2 * d][cc], 0x05040100);
      wf[cc & 1][1][d] = __builtin_amdgcn_perm(raw[s % WRING][2 * d + 1][cc], raw[s % WRING][2 * d][cc], 0x07060302);
    }
  };
  loadA(0); permW(0, 0);
  __builtin_amdgcn_sched_barrier(0);
  static_for<0, NSTEP * 4>([&](auto I) __attribute__((always_inline)) {
    constexpr int s = decltype(I)::value >> 2, cc = decltype(I)::value & 3;
    if constexpr (cc == 0 && s + WDEPTH < NSTEP) loadW(s + WDEPTH);
    if constexpr (cc == 1 && s + 1 < NSTEP) loadA(s + 1);
    if constexpr (cc == 2 && s == EPI_STEP) loadEpi();
    if constexpr (cc < 3) permW(s, cc + 1); else if constexpr (s + 1 < NSTEP) permW(s + 1, 0);
#pragma unroll
    for (int n = 0; n < 2; ++n)
#pragma unroll
      for (int t = 0; t < 4; ++t)
        acc[t][2 * cc + n] = __builtin_amdgcn_mfma_f32_16x16x32_bf16(__builtin_bit_cast(bf16x8, wf[cc & 1][n]), __builtin_bit_cast(bf16x8, af[s & 1][t]), acc[t][2 * cc + n], 0, 0, 0);
#pragma unroll
    for (int q = 0; q < 8; ++q) {
      __builtin_amdgcn_sched_group_barrier(0x8, 1, 0);
      __builtin_amdgcn_sched_group_barrier(0x2, 1, 0);
      if (cc == 0) __builtin_amdgcn_sched_group_barrier(0x20, 1, 0);
      if (cc == 2 && s == EPI_STEP) __builtin_amdgcn_sched_group_barrier(0x20, 2, 0);
      if (cc == 1 && q < 4) __builtin_amdgcn_sched_group_barrier(0x100, 1, 0);
    }
    __builtin_amdgcn_sched_barrier(0);
  });
  STAMP(3);
  __builtin_amdgcn_s_barrier();

#pragma unroll
  for (int t = 1; t < 4; ++t) {
    const int d = (w + t) & 3;
    LAS char* dst = lds + RED_OFF + (d * 3 + (t - 1)) * 8192 + lane * 16;
#pragma unroll
    for (int n = 0; n < 8; ++n) *(LAS f32x4*)(dst + n * 1024) = acc[t][n];
  }
  __builtin_amdgcn_s_waitcnt(0xC07F);
  __builtin_amdgcn_s_barrier();
  STAMP(4);
  float psum = 0.f;
  {
    LAS char* src = lds + RED_OFF + (w * 3) * 8192 + lane * 16;
#pragma unroll
    for (int n = 0; n < 8; ++n) {
      f32x4 v0 = *(LAS f32x4*)(src + n * 1024);
      f32x4 v1 = *(LAS f32x4*)(src + 8192 + n * 1024);
      f32x4 v2 = *(LAS f32x4*)(src + 16384 + n * 1024);
#pragma unroll
      for (int r = 0; r < 4; ++r) {
        float a = (acc[0][n][r] + v0[r]) + (v1[r] + v2[r]);
        const u32 bq = b1v[r][n >> 1], wq = w2v[r][n >> 1];
        const float bb = __uint_as_float((n & 1) ? (bq & 0xffff0000u) : (bq << 16));
        const float wv = __uint_as_float((n & 1) ? (wq & 0xffff0000u) : (wq << 16));
        float hv = rbf(rbf(a) + bb);
        hv = hv > 0.f ? hv : 0.f;
        psum = fmaf(hv, wv, psum);
      }
    }
  }
  float wsum = 0.f;
#pragma unroll
  for (int q = 0; q < 4; ++q) wsum = fmaf(bf2f(xv[q]), bf2f(wv4[q]), wsum);
  psum += __shfl_xor(psum, 16); wsum += __shfl_xor(wsum, 16);
  psum += __shfl_xor(psum, 32); wsum += __shfl_xor(wsum, 32);
  const float deep = rbf(rbf(psum) + b2f_);
  const float wide = rbf(rbf(wsum) + bwf_);
  const float z = rbf(wide + deep);
  if (g == 0) out[myrow] = __float2bfloat16(1.f / (1.f + __expf(-z)));
  STAMP(5);
}

extern "C" void kernel_launch(void* const* d_in, const int* in_sizes, int n_in, void* d_out, int out_size, void* d_ws,
                              size_t ws_size, hipStream_t stream) {
  (void)hipFuncSetAttribute((const void*)wd_main, hipFuncAttributeMaxDynamicSharedMemorySize, LDS_BYTES);
  wd_main<<<256, 256, LDS_BYTES, stream>>>((const int*)d_in[0], (const char*)d_in[1], (const unsigned short*)d_in[2],
                                           (const unsigned short*)d_in[3], (const unsigned short*)d_in[4],
                                           (const char*)d_in[5], (const char*)d_in[6], (const char*)d_in[7],
                                           (const unsigned short*)d_in[8], (__hip_bfloat16*)d_out
  );
}
```

```cpp
#include <hip/hip_runtime.h>
#include <hip/hip_bf16.h>
#include <stdint.h>
#pragma clang diagnostic ignored "-Wint-to-pointer-cast"
#pragma clang diagnostic ignored "-Winline-asm"

typedef unsigned int u32;
typedef unsigned long long u64;
typedef __attribute__((ext_vector_type(4))) unsigned u32x4;
typedef __attribute__((ext_vector_type(2))) unsigned u32x2;
typedef __attribute__((ext_vector_type(4))) float f32x4;
typedef __attribute__((ext_vector_type(2))) short s16x2;
typedef __attribute__((ext_vector_type(2))) float f32x2;
typedef __attribute__((ext_vector_type(2))) __bf16 bf16x2;
typedef __attribute__((ext_vector_type(8))) __bf16 bf16x8;
#define LAS __attribute__((address_space(3)))
#define GAS __attribute__((address_space(1)))
#define CAS __attribute__((address_space(4)))
typedef u32x2 u32x2_u __attribute__((aligned(2)));

constexpr int NB = 16384, NF = 100, NV = 1000, ND = 13;
constexpr int PLANE = 16416;
constexpr int RED_OFF = 3 * PLANE + 16000;
constexpr int IDX_OFF = RED_OFF + 512;
constexpr int LDS_BYTES = RED_OFF + 98304;

#define SB() __builtin_amdgcn_sched_barrier(0)
#define PIN(x) asm volatile("" : "+v"(x))
#define APIN(x) asm volatile("" : "+a"(x))
#define SPIN(x) asm volatile("" : "+s"(x))
#define LGKM0() __builtin_amdgcn_s_waitcnt(0xC07F)
#define VMFMA0(d, a, b, c) asm volatile("v_mfma_f32_16x16x32_bf16 %0, %1, %2, %3" : "=&v"(d) : "v"(a), "a"(b), "v"(c))
#define VMFMA(d, a, b) asm volatile("v_mfma_f32_16x16x32_bf16 %0, %1, %2, %0" : "+v"(d) : "v"(a), "a"(b))
#define AMFMA0(d, a, b) asm volatile("v_mfma_f32_16x16x32_bf16 %0, %1, %2, 0" : "=&a"(d) : "v"(a), "a"(b))
#define AMFMAB(d, a, b, c) asm volatile("v_mfma_f32_16x16x32_bf16 %0, %1, %2, %3" : "=&a"(d) : "v"(a), "a"(b), "a"(c))
#define AMFMA(d, a, b) asm volatile("v_mfma_f32_16x16x32_bf16 %0, %1, %2, %0" : "+a"(d) : "v"(a), "a"(b))
#define MFMA(a, b, c) __builtin_amdgcn_mfma_f32_16x16x32_bf16(__builtin_bit_cast(bf16x8, a), __builtin_bit_cast(bf16x8, b), c, 0, 0, 0)

__device__ __forceinline__ float bf2f(u32 x) { return __uint_as_float(x << 16); }
__device__ __forceinline__ float rbf(float x) { return __bfloat162float(__float2bfloat16(x)); }
__device__ __forceinline__ float sum16(float x) {
  const auto s = __builtin_amdgcn_permlane16_swap(__float_as_uint(x), __float_as_uint(x), false, false);
  return __uint_as_float(s[0]) + __uint_as_float(s[1]);
}
__device__ __forceinline__ float sum32(float x) {
  const auto s = __builtin_amdgcn_permlane32_swap(__float_as_uint(x), __float_as_uint(x), false, false);
  return __uint_as_float(s[0]) + __uint_as_float(s[1]);
}
__device__ __forceinline__ u32 pack2(float lo, float hi) {
  return __builtin_bit_cast(u32, __builtin_convertvector(f32x2{lo, hi}, bf16x2));
}
__device__ __forceinline__ u32 relu2(u32 p) {
  const s16x2 z = {0, 0};
  return __builtin_bit_cast(u32, __builtin_elementwise_max(__builtin_bit_cast(s16x2, p), z));
}

__global__ __launch_bounds__(256, 1) void wd_main(const int* __restrict__ idx, const char* __restrict__ table,
                                                  const char* __restrict__ W1, char* __restrict__ ws,
                                                  const char* __restrict__ xw, const char* __restrict__ ww,
                                                  const char* __restrict__ b1, const char* __restrict__ W2,
                                                  const unsigned short* __restrict__ bw, const unsigned short* __restrict__ b2,
                                                  __hip_bfloat16* __restrict__ out
) {
  extern __shared__ __attribute__((aligned(16))) char smem[];
  const u32 lb = (u32)(uintptr_t)(LAS char*)smem;
  const int tid = threadIdx.x;
  const int lane = tid & 63;
  const int w = __builtin_amdgcn_readfirstlane(tid >> 6);
  const int c16 = lane & 15, g = lane >> 4;
  u32 lane16 = lane * 16;
  const int bid = blockIdx.x;
  const int j32 = bid >> 3;
  const int half = j32 & 1, pj = j32 >> 1;
  const int b0 = (pj * 8 + (bid & 7)) * 128;
  const int r4 = pj & 3;
  const int moff0 = r4 * 2048, moff1 = ((r4 + 1) & 3) * 2048, moff2 = (r4 ^ 2) * 2048, moff3 = ((r4 + 3) & 3) * 2048;

  u32 pfv;
  {
    u64 pc; u32 len;
    asm volatile("s_getpc_b64 %0\n.Lwd_pc:\n\ts_mov_b32 %1, .Lwd_end-.Lwd_pc\n\ts_max_u32 %1, %1, .Lwd_end2-.Lwd_pc" : "=s"(pc), "=s"(len) :: "scc");
    const u32x4 rC = {(u32)pc, (u32)(pc >> 32) & 0xffffu, len, 0x00020000u};
    asm volatile("buffer_load_dword %0, %1, %2, 0 offen" : "=v"(pfv) : "v"((tid * 32 + j32) * 128), "s"(rC) : "memory");
  }
  u32 cho = c16 * 4, ibo = c16 * 8;
  u32 voffA = (u32)(128 * (4 * w + g)) * 256u + half * 128 + cho;
  const int tp0 = w + 2 * j32;
  u32 lane16b = lane16 + 4096;
  u32 hoff = (half * 64 + 8 * g) * 2;
  PIN(voffA); PIN(lane16b); PIN(hoff); PIN(ibo); PIN(cho);
  const f32x4 zero4 = {0.f, 0.f, 0.f, 0.f};
#define ADDRS() \
  u32 voffB = (u32)(2048 + 64 * (4 * w + g)) * 256u + half * 128 + cho; \
  u32 voffE = (u32)(32 * (96 + w) + 8 * ((g + r4) & 3)) * 256u + half * 128 + cho; \
  const int myrow = b0 + 64 * half + 16 * w + c16; \
  u32 oo = myrow * 2; \
  u32 wo = g == 3 ? 18 : 8 * g;                   \
  u32 xo = __umul24(myrow, ND * 2) + wo; \
  u32 wm0 = g == 3 ? 0u : ~0u, wm1 = wm0 | 0xffff0000u; \
  PIN(wm0); PIN(wm1); \
  u32 gplane = lb + (u32)g * PLANE; \
  u32 ta = lb + (u32)(((lane & 3) - r4) & 3) * PLANE + (u32)(lane >> 2) * 16; \
  u32 wa0 = lane16 + (lb + RED_OFF + (u32)((w + 1) & 3) * 12288u); \
  u32 wa1 = lane16 + (lb + RED_OFF + 4096 + (u32)((w + 2) & 3) * 12288u); \
  u32 wa2 = lane16 + (lb + RED_OFF + 8192 + (u32)((w + 3) & 3) * 12288u); \
  u32 ra = lane16 + (lb + RED_OFF + (u32)w * 12288u); \
  const u32 i4 = lb + IDX_OFF + __umul24(c16, 400u) + 64 * w + 16 * g; \
  u32 ja0 = i4 + (u32)((w + 1) & 3) * 6400u, ja1 = i4 + (u32)((w + 2) & 3) * 6400u, ja2 = i4 + (u32)((w + 3) & 3) * 6400u, ja3 = i4 + (u32)w * 6400u; \
  u32 d2 = 256 - 32 * w - 8 * g, d1 = 384 - 60 * w - 16 * g; \
  PIN(voffB); PIN(voffE); PIN(oo); PIN(wo); PIN(xo); PIN(gplane); PIN(ta); \
  PIN(wa0); PIN(wa1); PIN(wa2); PIN(ra); PIN(ja0); PIN(ja1); PIN(ja2); PIN(ja3); PIN(d2); PIN(d1); \
  const char* pbase = ws + (size_t)((bid ^ 8) * 4 + w) * 256; \
  const int* isrc = idx + (size_t)(b0 + 64 * (1 - half) + 16 * w) * NF; \
  const u32 idst = lb + IDX_OFF + 6400 * w
  SB();
  asm volatile("" :: "s"(idx), "s"(table), "s"(W1), "s"(ws), "s"(xw), "s"(ww), "s"(b1), "s"(W2), "s"(bw), "s"(b2), "s"(out));
  u32 b2r = *(const CAS u32*)((uintptr_t)b2 & ~(uintptr_t)3), bwr = *(const CAS u32*)((uintptr_t)bw & ~(uintptr_t)3);
  const u32x4 rT = {(u32)(uintptr_t)table, (u32)((uintptr_t)table >> 32) & 0xffffu, NV * 64, 0x00020000u};
  const char* ibase = ws + (size_t)(bid * 4 + w) * 256;
#define outbox ((GAS u64*)(pbase + ibo))
#define chal_peer ((GAS u32*)(pbase + 128 + cho))
#define POLL(x) asm volatile("global_load_dwordx2 %0, %1, %2 sc1" : "=v"(x) : "v"(ibo), "s"(ibase) : "memory")
#define GLD(d, vo, sb, off) asm volatile("global_load_dword %0, %1, %2 offset:" #off : "=v"(d) : "v"(vo), "s"(sb) : "memory")
#define GLD4(d, vo, sb, off) asm volatile("global_load_dwordx4 %0, %1, %2 offset:" #off : "=v"(d) : "v"(vo), "s"(sb) : "memory")
#define TLD(d, i) asm volatile("buffer_load_dwordx4 %0, %1, %2, %3 offen" : "=v"(d) : "v"(lane16), "s"(rT), "s"(((tp0 + 4 * (i)) & 63) * 1024) : "memory")
#define TWR(d, i) *(LAS u32x4*)(ta + ((tp0 + 4 * (i)) & 63) * 256) = d
#define IDMA(row0, dst) \
  asm volatile("s_mov_b32 m0, %3\n\ts_nop 0\n\t" \
               "global_load_lds_dwordx4 %0, %2 nt\n\tglobal_load_lds_dwordx4 %0, %2 offset:1024 nt\n\t" \
               "global_load_lds_dwordx4 %0, %2 offset:2048 nt\n\tglobal_load_lds_dwordx4 %0, %2 offset:3072 nt\n\t" \
               "s_add_u32 m0, %3, 0x1000\n\ts_nop 0\n\t" \
               "global_load_lds_dwordx4 %1, %2 nt\n\tglobal_load_lds_dwordx4 %1, %2 offset:1024 nt\n\t" \
               "s_mov_b64 exec, 0xffff\n\tglobal_load_lds_dwordx4 %1, %2 offset:2048 nt\n\ts_mov_b64 exec, -1" \
               :: "v"(lane16), "v"(lane16b), "s"(idx + (size_t)(row0) * NF), "s"(dst) : "m0", "scc", "memory")
#define IKEEP() IDMA(b0 + 64 * half + 16 * w, lb + IDX_OFF + 25600 + 6400 * w)
#define ISEND1(k) do { \
  if ((k) < 6) asm volatile("s_mov_b32 m0, %2\n\ts_nop 0\n\tglobal_load_lds_dwordx4 %0, %1 offset:%c3 nt" \
               :: "v"((k) < 4 ? lane16 : lane16b), "s"(isrc), "s"(idst + ((k) < 4 ? 0 : 4096)), "i"(((k) & 3) * 1024) : "m0", "memory"); \
  else asm volatile("s_mov_b32 m0, %2\n\ts_mov_b64 exec, 0xffff\n\tglobal_load_lds_dwordx4 %0, %1 offset:2048 nt\n\ts_mov_b64 exec, -1" \
               :: "v"(lane16b), "s"(isrc), "s"(idst + 4096) : "m0", "memory"); } while (0)
#define BIAS() \
  f32x4 bias0, bias1, bias2, bias3; \
  _Pragma("unroll") for (int r = 0; r < 4; ++r) { \
    bias0[r] = __uint_as_float(b1v0[r] << 16); bias1[r] = __uint_as_float(b1v0[r] & 0xffff0000u); \
    bias2[r] = __uint_as_float(b1v1[r] << 16); bias3[r] = __uint_as_float(b1v1[r] & 0xffff0000u); \
  } \
  PIN(bias0); PIN(bias1); PIN(bias2); PIN(bias3)
#define IV0(t) const u32 iv0_##t = *(LAS u32*)((((t) & 3) == 0 ? ja0 : ((t) & 3) == 1 ? ja1 : ((t) & 3) == 2 ? ja2 : ja3) + ((t) >> 2) * 25600);
#define ADT(t) u32 ad0_##t = (iv0_##t << 4) + lb; PIN(ad0_##t);
  u32x4 wf0_0, wf0_1, wf0_2, wf0_3;
  u32x4 wf1_0, wf1_1, wf1_2, wf1_3;
  u32x4 wf2_0, wf2_1, wf2_2, wf2_3;
  u32x4 wf3_0, wf3_1, wf3_2, wf3_3;
  u32x4 wf4_0, wf4_1, wf4_2, wf4_3;
  u32x4 wf5_0, wf5_1, wf5_2, wf5_3;
  u32x4 wf6_0, wf6_1, wf6_2, wf6_3;
  u32x4 wf7_0, wf7_1, wf7_2, wf7_3;
  u32x4 wf8_0, wf8_1, wf8_2, wf8_3;
  u32x4 wf9_0, wf9_1, wf9_2, wf9_3;
  u32x4 wf10_0, wf10_1, wf10_2, wf10_3;
  u32x4 wf11_0, wf11_1, wf11_2, wf11_3;
  u32x4 wf12_0, wf12_1, wf12_2, wf12_3;
  u32x4 wf13_0, wf13_1, wf13_2, wf13_3;
  u32x4 wf14_0, wf14_1, wf14_2, wf14_3;
  u32x4 wf15_0, wf15_1, wf15_2, wf15_3;
  u32x4 wf16_0, wf16_1, wf16_2, wf16_3;
  u32x4 wf17_0, wf17_1, wf17_2, wf17_3;
  u32x4 wf18_0, wf18_1, wf18_2, wf18_3;
  u32x4 wf19_0, wf19_1, wf19_2, wf19_3;
  u32x4 wf20_0, wf20_1, wf20_2, wf20_3;
  u32x4 wf21_0, wf21_1, wf21_2, wf21_3;
  u32x4 wf22_0, wf22_1, wf22_2, wf22_3;
  u32x4 wf23_0, wf23_1, wf23_2, wf23_3;
  u32x4 wf24_0, wf24_1, wf24_2, wf24_3;
  u32x4 q3_0, q3_1, q7_0, q7_1;
  u32 wo0 = moff0 + 0; SPIN(wo0); const char* wb0 = W1 + wo0; SB();
  u32 r0_0; GLD(r0_0, voffA, wb0, 0); SB();
  u32 r0_1; GLD(r0_1, voffA, wb0, 64); SB();
  u32 r0_2; GLD(r0_2, voffA, wb0, 256); SB();
  u32 r0_3; GLD(r0_3, voffA, wb0, 320); SB();
  u32 r0_4; GLD(r0_4, voffA, wb0, 512); SB();
  u32 r0_5; GLD(r0_5, voffA, wb0, 576); SB();
  u32 r0_6; GLD(r0_6, voffA, wb0, 768); SB();
  u32 r0_7; GLD(r0_7, voffA, wb0, 832); SB();
  u32 r0_8; GLD(r0_8, voffA, wb0, 1024); SB();
  u32 r0_9; GLD(r0_9, voffA, wb0, 1088); SB();
  u32 r0_10; GLD(r0_10, voffA, wb0, 1280); SB();
  u32 r0_11; GLD(r0_11, voffA, wb0, 1344); SB();
  u32 r0_12; GLD(r0_12, voffA, wb0, 1536); SB();
  u32 r0_13; GLD(r0_13, voffA, wb0, 1600); SB();
  u32 r0_14; GLD(r0_14, voffA, wb0, 1792); SB();
  u32 r0_15; GLD(r0_15, voffA, wb0, 1856); SB();
  u32x4 tb0; TLD(tb0, 0); SB();
  u32x4 b1v0; GLD4(b1v0, hoff, b1, 0); SB();
  u32x4 b1v1; GLD4(b1v1, hoff, b1, 64); SB();
  u32 stale; asm volatile("global_load_dword %0, %1, %2 offset:4 sc1" : "=v"(stale) : "v"(ibo), "s"(ibase) : "memory");
  IKEEP(); SB();
  u32x4 tb1; TLD(tb1, 1); SB();
  u32x4 tb2; TLD(tb2, 2); SB();
  u32x4 tb3; TLD(tb3, 3); SB();
  ADDRS(); SB();
  asm volatile("s_waitcnt vmcnt(14)" : "+v"(pfv), "+v"(r0_0), "+v"(r0_1), "+v"(r0_2), "+v"(r0_3), "+v"(r0_4), "+v"(r0_5), "+v"(r0_6), "+v"(r0_7), "+v"(r0_8), "+v"(r0_9), "+v"(r0_10), "+v"(r0_11), "+v"(r0_12), "+v"(r0_13), "+v"(r0_14), "+v"(r0_15) :: "memory"); SB();
  wf0_0[0] = __builtin_amdgcn_perm(r0_2, r0_0, 0x05040100); SB();
  wf0_1[0] = __builtin_amdgcn_perm(r0_2, r0_0, 0x07060302); SB();
  wf0_2[0] = __builtin_amdgcn_perm(r0_3, r0_1, 0x05040100); SB();
  wf0_3[0] = __builtin_amdgcn_perm(r0_3, r0_1, 0x07060302); SB();
  wf0_0[1] = __builtin_amdgcn_perm(r0_6, r0_4, 0x05040100); SB();
  wf0_1[1] = __builtin_amdgcn_perm(r0_6, r0_4, 0x07060302); SB();
  wf0_2[1] = __builtin_amdgcn_perm(r0_7, r0_5, 0x05040100); SB();
  wf0_3[1] = __builtin_amdgcn_perm(r0_7, r0_5, 0x07060302); SB();
  wf0_0[2] = __builtin_amdgcn_perm(r0_10, r0_8, 0x05040100); SB();
  wf0_1[2] = __builtin_amdgcn_perm(r0_10, r0_8, 0x07060302); SB();
  wf0_2[2] = __builtin_amdgcn_perm(r0_11, r0_9, 0x05040100); SB();
  wf0_3[2] = __builtin_amdgcn_perm(r0_11, r0_9, 0x07060302); SB();
  wf0_0[3] = __builtin_amdgcn_perm(r0_14, r0_12, 0x05040100); SB();
  wf0_1[3] = __builtin_amdgcn_perm(r0_14, r0_12, 0x07060302); SB();
  wf0_2[3] = __builtin_amdgcn_perm(r0_15, r0_13, 0x05040100); SB();
  wf0_3[3] = __builtin_amdgcn_perm(r0_15, r0_13, 0x07060302); SB();
  PIN(wf0_0); PIN(wf0_1); PIN(wf0_2); PIN(wf0_3); SB();
  asm volatile("s_waitcnt vmcnt(13)" : "+v"(tb0) :: "memory"); SB();
  TWR(tb0, 0); SB();
  asm volatile("s_waitcnt vmcnt(10)" : "+v"(b1v0), "+v"(b1v1), "+v"(stale) :: "memory"); SB();
  BIAS(); SB();
  u32 mych = ((stale >> 16) + 1u) & 0xffffu;
  u32x4 tb4; TLD(tb4, 4); SB();
  ISEND1(0); SB();
  ISEND1(1); SB();
  ISEND1(2); SB();
  ISEND1(3); SB();
  ISEND1(4); SB();
  ISEND1(5); SB();
  ISEND1(6); SB();
  u32x4 tb5; TLD(tb5, 5); SB();
  asm volatile("s_waitcnt vmcnt(11)" : "+v"(tb1) :: "memory"); SB();
  TWR(tb1, 1); SB();
  u32x4 tb6; TLD(tb6, 6); SB();
  asm volatile("s_waitcnt vmcnt(11)" : "+v"(tb2) :: "memory"); SB();
  TWR(tb2, 2); SB();
  u32x4 tb7; TLD(tb7, 7); SB();
  asm volatile("s_waitcnt vmcnt(11)" : "+v"(tb3) :: "memory"); SB();
  TWR(tb3, 3); SB();
  u32x4 tb8; TLD(tb8, 8); SB();
  asm volatile("s_waitcnt vmcnt(11)" : "+v"(tb4) :: "memory"); SB();
  TWR(tb4, 4); SB();
  u32x4 tb9; TLD(tb9, 9); SB();
  u32x4 tb10; TLD(tb10, 10); SB();
  u32x4 tb11; TLD(tb11, 11); SB();
  u32x4 tb12; TLD(tb12, 12); SB();
  u32x4 tb13; TLD(tb13, 13); SB();
  u32x4 tb14; TLD(tb14, 14); SB();
  u32x4 tb15; TLD(tb15, 15); SB();
  SPIN(b2r); SPIN(bwr); b2r >>= ((u32)(uintptr_t)b2 & 2u) * 8u; bwr >>= ((u32)(uintptr_t)bw & 2u) * 8u; SPIN(b2r); SPIN(bwr); SB();
  asm volatile("s_waitcnt vmcnt(11)\n\ts_barrier" :  :: "memory"); SB();
  IV0(0) IV0(4) IV0(1) IV0(5) IV0(2) IV0(6) IV0(3) IV0(7) SB();
  u32 wo1 = moff1 + 0; SPIN(wo1); const char* wb1 = W1 + wo1; SB();
  const u32 r1_0 = *(const u32*)(wb1 + voffA + 0); SB();
  const u32 r1_1 = *(const u32*)(wb1 + voffA + 64); SB();
  const u32 r1_2 = *(const u32*)(wb1 + voffA + 256); SB();
  const u32 r1_3 = *(const u32*)(wb1 + voffA + 320); SB();
  asm volatile("s_waitcnt vmcnt(14)" : "+v"(tb5) :: "memory"); SB();
  TWR(tb5, 5); SB();
  const u32 r1_4 = *(const u32*)(wb1 + voffA + 512); SB();
  const u32 r1_5 = *(const u32*)(wb1 + voffA + 576); SB();
  const u32 r1_6 = *(const u32*)(wb1 + voffA + 768); SB();
  const u32 r1_7 = *(const u32*)(wb1 + voffA + 832); SB();
  asm volatile("s_waitcnt vmcnt(17)" : "+v"(tb6) :: "memory"); SB();
  TWR(tb6, 6); SB();
  const u32 r1_8 = *(const u32*)(wb1 + voffA + 1024); SB();
  const u32 r1_9 = *(const u32*)(wb1 + voffA + 1088); SB();
  const u32 r1_10 = *(const u32*)(wb1 + voffA + 1280); SB();
  const u32 r1_11 = *(const u32*)(wb1 + voffA + 1344); SB();
  asm volatile("s_waitcnt vmcnt(20)" : "+v"(tb7) :: "memory"); SB();
  TWR(tb7, 7); SB();
  ADT(0) ADT(1) ADT(2) ADT(3) ADT(4) ADT(5) ADT(6) ADT(7) SB();
  const u32 r1_12 = *(const u32*)(wb1 + voffA + 1536); SB();
  const u32 r1_13 = *(const u32*)(wb1 + voffA + 1600); SB();
  const u32 r1_14 = *(const u32*)(wb1 + voffA + 1792); SB();
  const u32 r1_15 = *(const u32*)(wb1 + voffA + 1856); SB();
  asm volatile("s_waitcnt vmcnt(23)" : "+v"(tb8) :: "memory"); SB();
  TWR(tb8, 8); SB();
  u32 wo2 = moff2 + 0; SPIN(wo2); const char* wb2 = W1 + wo2; SB();
  const u32 r2_0 = *(const u32*)(wb2 + voffA + 0); SB();
  const u32 r2_1 = *(const u32*)(wb2 + voffA + 64); SB();
  const u32 r2_2 = *(const u32*)(wb2 + voffA + 256); SB();
  const u32 r2_3 = *(const u32*)(wb2 + voffA + 320); SB();
  asm volatile("s_waitcnt vmcnt(26)" : "+v"(tb9) :: "memory"); SB();
  TWR(tb9, 9); SB();
  const u32 r2_4 = *(const u32*)(wb2 + voffA + 512); SB();
  const u32 r2_5 = *(const u32*)(wb2 + voffA + 576); SB();
  const u32 r2_6 = *(const u32*)(wb2 + voffA + 768); SB();
  const u32 r2_7 = *(const u32*)(wb2 + voffA + 832); SB();
  asm volatile("s_waitcnt vmcnt(29)" : "+v"(tb10) :: "memory"); SB();
  TWR(tb10, 10); SB();
  const u32 r2_8 = *(const u32*)(wb2 + voffA + 1024); SB();
  const u32 r2_9 = *(const u32*)(wb2 + voffA + 1088); SB();
  const u32 r2_10 = *(const u32*)(wb2 + voffA + 1280); SB();
  const u32 r2_11 = *(const u32*)(wb2 + voffA + 1344); SB();
  asm volatile("s_waitcnt vmcnt(32)" : "+v"(tb11) :: "memory"); SB();
  TWR(tb11, 11); SB();
  const u32 r2_12 = *(const u32*)(wb2 + voffA + 1536); SB();
  const u32 r2_13 = *(const u32*)(wb2 + voffA + 1600); SB();
  const u32 r2_14 = *(const u32*)(wb2 + voffA + 1792); SB();
  const u32 r2_15 = *(const u32*)(wb2 + voffA + 1856); SB();
  asm volatile("s_waitcnt vmcnt(35)" : "+v"(tb12) :: "memory"); SB();
  TWR(tb12, 12); SB();
  asm volatile("s_waitcnt vmcnt(34)" : "+v"(tb13) :: "memory"); SB();
  TWR(tb13, 13); SB();
  asm volatile("s_waitcnt vmcnt(33)" : "+v"(tb14) :: "memory"); SB();
  TWR(tb14, 14); SB();
  asm volatile("s_waitcnt vmcnt(32)" : "+v"(tb15) :: "memory"); SB();
  TWR(tb15, 15); SB();
  asm volatile("global_store_dword %0, %1, %2 offset:128 sc1" :: "v"(cho), "v"(mych), "s"(ibase) : "memory"); SB();
  LGKM0(); __builtin_amdgcn_s_barrier(); SB();
  const u32x4 af0_0 = *(LAS u32x4*)(ad0_0 + 0 * PLANE); SB();
  const u32x4 af0_1 = *(LAS u32x4*)(ad0_1 + 0 * PLANE); SB();
  const u32x4 af0_2 = *(LAS u32x4*)(ad0_2 + 0 * PLANE); SB();
  const u32x4 af0_3 = *(LAS u32x4*)(ad0_3 + 0 * PLANE); SB();
  const u32x4 af0_4 = *(LAS u32x4*)(ad0_4 + 0 * PLANE); SB();
  const u32x4 af0_5 = *(LAS u32x4*)(ad0_5 + 0 * PLANE); SB();
  const u32x4 af0_6 = *(LAS u32x4*)(ad0_6 + 0 * PLANE); SB();
  const u32x4 af0_7 = *(LAS u32x4*)(ad0_7 + 0 * PLANE); SB();
  f32x4 acc0_0; AMFMA0(acc0_0, wf0_0, af0_0); SB();
  u32 wo3 = moff3 + 0; SPIN(wo3); const char* wb3 = W1 + wo3; const u32 r3_0 = *(const u32*)(wb3 + voffA + 0); SB();
  f32x4 acc0_1; AMFMA0(acc0_1, wf0_1, af0_0); SB();
  const u32x4 af1_0 = *(LAS u32x4*)(ad0_0 + 1 * PLANE); const u32 r3_1 = *(const u32*)(wb3 + voffA + 64); SB();
  f32x4 acc0_2; AMFMA0(acc0_2, wf0_2, af0_0); SB();
  wf1_0[0] = __builtin_amdgcn_perm(r1_2, r1_0, 0x05040100); wf1_1[0] = __builtin_amdgcn_perm(r1_2, r1_0, 0x07060302); SB();
  f32x4 acc0_3; AMFMA0(acc0_3, wf0_3, af0_0); SB();
  const u32 r3_2 = *(const u32*)(wb3 + voffA + 256); const u32x4 iv4_0 = *(volatile LAS u32x4*)(ja0 + 0); SB();
  f32x4 acc1_0; AMFMA0(acc1_0, wf0_0, af0_1); SB();
  const u32 r3_3 = *(const u32*)(wb3 + voffA + 320); SB();
  f32x4 acc1_1; AMFMA0(acc1_1, wf0_1, af0_1); SB();
  const u32x4 af1_1 = *(LAS u32x4*)(ad0_1 + 1 * PLANE); const u32 r3_4 = *(const u32*)(wb3 + voffA + 512); SB();
  f32x4 acc1_2; AMFMA0(acc1_2, wf0_2, af0_1); SB();
  wf1_2[0] = __builtin_amdgcn_perm(r1_3, r1_1, 0x05040100); wf1_3[0] = __builtin_amdgcn_perm(r1_3, r1_1, 0x07060302); SB();
  f32x4 acc1_3; AMFMA0(acc1_3, wf0_3, af0_1); SB();
  const u32 r3_5 = *(const u32*)(wb3 + voffA + 576); SB();
  f32x4 acc2_0; AMFMA0(acc2_0, wf0_0, af0_2); SB();
  const u32 r3_6 = *(const u32*)(wb3 + voffA + 768); SB();
  f32x4 acc2_1; AMFMA0(acc2_1, wf0_1, af0_2); SB();
  const u32x4 af1_2 = *(LAS u32x4*)(ad0_2 + 1 * PLANE); const u32 r3_7 = *(const u32*)(wb3 + voffA + 832); SB();
  f32x4 acc2_2; AMFMA0(acc2_2, wf0_2, af0_2); SB();
  wf1_0[1] = __builtin_amdgcn_perm(r1_6, r1_4, 0x05040100); wf1_1[1] = __builtin_amdgcn_perm(r1_6, r1_4, 0x07060302); SB();
  f32x4 acc2_3; AMFMA0(acc2_3, wf0_3, af0_2); SB();
  const u32 r3_8 = *(const u32*)(wb3 + voffA + 1024); const u32x4 iv4_1 = *(volatile LAS u32x4*)(ja1 + 0); SB();
  f32x4 acc3_0; VMFMA0(acc3_0, wf0_0, af0_3, bias0); SB();
  const u32 r3_9 = *(const u32*)(wb3 + voffA + 1088); SB();
  f32x4 acc3_1; VMFMA0(acc3_1, wf0_1, af0_3, bias1); SB();
  const u32x4 af1_3 = *(LAS u32x4*)(ad0_3 + 1 * PLANE); const u32 r3_10 = *(const u32*)(wb3 + voffA + 1280); SB();
  f32x4 acc3_2; VMFMA0(acc3_2, wf0_2, af0_3, bias2); SB();
  wf1_2[1] = __builtin_amdgcn_perm(r1_7, r1_5, 0x05040100); wf1_3[1] = __builtin_amdgcn_perm(r1_7, r1_5, 0x07060302); SB();
  f32x4 acc3_3; VMFMA0(acc3_3, wf0_3, af0_3, bias3); SB();
  const u32 r3_11 = *(const u32*)(wb3 + voffA + 1344); SB();
  f32x4 acc4_0; AMFMA0(acc4_0, wf0_0, af0_4); SB();
  const u32 r3_12 = *(const u32*)(wb3 + voffA + 1536); SB();
  f32x4 acc4_1; AMFMA0(acc4_1, wf0_1, af0_4); SB();
  const u32x4 af1_4 = *(LAS u32x4*)(ad0_4 + 1 * PLANE); const u32 r3_13 = *(const u32*)(wb3 + voffA + 1600); SB();
  f32x4 acc4_2; AMFMA0(acc4_2, wf0_2, af0_4); SB();
  wf1_0[2] = __builtin_amdgcn_perm(r1_10, r1_8, 0x05040100); wf1_1[2] = __builtin_amdgcn_perm(r1_10, r1_8, 0x07060302); SB();
  f32x4 acc4_3; AMFMA0(acc4_3, wf0_3, af0_4); SB();
  const u32 r3_14 = *(const u32*)(wb3 + voffA + 1792); const u32x4 iv4_2 = *(volatile LAS u32x4*)(ja2 + 0); SB();
  f32x4 acc5_0; AMFMA0(acc5_0, wf0_0, af0_5); SB();
  const u32 r3_15 = *(const u32*)(wb3 + voffA + 1856); SB();
  f32x4 acc5_1; AMFMA0(acc5_1, wf0_1, af0_5); SB();
  const u32x4 af1_5 = *(LAS u32x4*)(ad0_5 + 1 * PLANE); u32 wo4 = moff0 + 8192; SPIN(wo4); const char* wb4 = W1 + wo4; const u32 r4_0 = *(const u32*)(wb4 + voffA + 0); SB();
  f32x4 acc5_2; AMFMA0(acc5_2, wf0_2, af0_5); SB();
  wf1_2[2] = __builtin_amdgcn_perm(r1_11, r1_9, 0x05040100); wf1_3[2] = __builtin_amdgcn_perm(r1_11, r1_9, 0x07060302); SB();
  f32x4 acc5_3; AMFMA0(acc5_3, wf0_3, af0_5); SB();
  const u32 r4_1 = *(const u32*)(wb4 + voffA + 64); SB();
  f32x4 acc6_0; AMFMA0(acc6_0, wf0_0, af0_6); SB();
  const u32 r4_2 = *(const u32*)(wb4 + voffA + 256); SB();
  f32x4 acc6_1; AMFMA0(acc6_1, wf0_1, af0_6); SB();
  const u32x4 af1_6 = *(LAS u32x4*)(ad0_6 + 1 * PLANE); const u32 r4_3 = *(const u32*)(wb4 + voffA + 320); SB();
  f32x4 acc6_2; AMFMA0(acc6_2, wf0_2, af0_6); SB();
  wf1_0[3] = __builtin_amdgcn_perm(r1_14, r1_12, 0x05040100); wf1_1[3] = __builtin_amdgcn_perm(r1_14, r1_12, 0x07060302); SB();
  f32x4 acc6_3; AMFMA0(acc6_3, wf0_3, af0_6); SB();
  const u32 r4_4 = *(const u32*)(wb4 + voffA + 512); const u32x4 iv4_3 = *(volatile LAS u32x4*)(ja3 + 0); SB();
  f32x4 acc7_0; VMFMA0(acc7_0, wf0_0, af0_7, bias0); SB();
  const u32 r4_5 = *(const u32*)(wb4 + voffA + 576); SB();
  f32x4 acc7_1; VMFMA0(acc7_1, wf0_1, af0_7, bias1); SB();
  const u32x4 af1_7 = *(LAS u32x4*)(ad0_7 + 1 * PLANE); const u32 r4_6 = *(const u32*)(wb4 + voffA + 768); SB();
  f32x4 acc7_2; VMFMA0(acc7_2, wf0_2, af0_7, bias2); SB();
  wf1_2[3] = __builtin_amdgcn_perm(r1_15, r1_13, 0x05040100); wf1_3[3] = __builtin_amdgcn_perm(r1_15, r1_13, 0x07060302); SB();
  f32x4 acc7_3; VMFMA0(acc7_3, wf0_3, af0_7, bias3); SB();
  const u32 r4_7 = *(const u32*)(wb4 + voffA + 832); SB();
  __builtin_amdgcn_s_waitcnt(0x8970);
  AMFMA(acc0_0, wf1_0, af1_0); SB();
  const u32 r4_8 = *(const u32*)(wb4 + voffA + 1024); wf2_0[0] = __builtin_amdgcn_perm(r2_2, r2_0, 0x05040100); SB();
  AMFMA(acc0_1, wf1_1, af1_0); SB();
  const u32x4 af2_0 = *(LAS u32x4*)(ad0_0 + 2 * PLANE); const u32 r4_9 = *(const u32*)(wb4 + voffA + 1088); SB();
  AMFMA(acc0_2, wf1_2, af1_0); SB();
  wf2_1[0] = __builtin_amdgcn_perm(r2_2, r2_0, 0x07060302); wf2_2[0] = __builtin_amdgcn_perm(r2_3, r2_1, 0x05040100); SB();
  AMFMA(acc0_3, wf1_3, af1_0); SB();
  const u32 r4_10 = *(const u32*)(wb4 + voffA + 1280); wf2_3[0] = __builtin_amdgcn_perm(r2_3, r2_1, 0x07060302); SB();
  AMFMA(acc1_0, wf1_0, af1_1); SB();
  const u32 r4_11 = *(const u32*)(wb4 + voffA + 1344); wf2_0[1] = __builtin_amdgcn_perm(r2_6, r2_4, 0x05040100); SB();
  AMFMA(acc1_1, wf1_1, af1_1); SB();
  const u32x4 af3_0 = *(LAS u32x4*)(ad0_0 + 3 * PLANE); const u32 r4_12 = *(const u32*)(wb4 + voffA + 1536); SB();
  AMFMA(acc1_2, wf1_2, af1_1); SB();
  wf2_1[1] = __builtin_amdgcn_perm(r2_6, r2_4, 0x07060302); wf2_2[1] = __builtin_amdgcn_perm(r2_7, r2_5, 0x05040100); SB();
  AMFMA(acc1_3, wf1_3, af1_1); SB();
  const u32 r4_13 = *(const u32*)(wb4 + voffA + 1600); wf2_3[1] = __builtin_amdgcn_perm(r2_7, r2_5, 0x07060302); SB();
  __builtin_amdgcn_s_waitcnt(0x487e);
  AMFMA(acc2_0, wf1_0, af1_2); SB();
  const u32 r4_14 = *(const u32*)(wb4 + voffA + 1792); wf2_0[2] = __builtin_amdgcn_perm(r2_10, r2_8, 0x05040100); SB();
  AMFMA(acc2_1, wf1_1, af1_2); SB();
  const u32x4 af2_1 = *(LAS u32x4*)(ad0_1 + 2 * PLANE); const u32 r4_15 = *(const u32*)(wb4 + voffA + 1856); SB();
  AMFMA(acc2_2, wf1_2, af1_2); SB();
  wf2_1[2] = __builtin_amdgcn_perm(r2_10, r2_8, 0x07060302); wf2_2[2] = __builtin_amdgcn_perm(r2_11, r2_9, 0x05040100); SB();
  AMFMA(acc2_3, wf1_3, af1_2); SB();
  u32 wo5 = moff1 + 8192; SPIN(wo5); const char* wb5 = W1 + wo5; const u32 r5_0 = *(const u32*)(wb5 + voffA + 0); wf2_3[2] = __builtin_amdgcn_perm(r2_11, r2_9, 0x07060302); SB();
  VMFMA(acc3_0, wf1_0, af1_3); SB();
  const u32 r5_1 = *(const u32*)(wb5 + voffA + 64); wf2_0[3] = __builtin_amdgcn_perm(r2_14, r2_12, 0x05040100); SB();
  VMFMA(acc3_1, wf1_1, af1_3); SB();
  const u32x4 af3_1 = *(LAS u32x4*)(ad0_1 + 3 * PLANE); const u32 r5_2 = *(const u32*)(wb5 + voffA + 256); SB();
  VMFMA(acc3_2, wf1_2, af1_3); SB();
  wf2_1[3] = __builtin_amdgcn_perm(r2_14, r2_12, 0x07060302); wf2_2[3] = __builtin_amdgcn_perm(r2_15, r2_13, 0x05040100); SB();
  VMFMA(acc3_3, wf1_3, af1_3); SB();
  const u32 r5_3 = *(const u32*)(wb5 + voffA + 320); wf2_3[3] = __builtin_amdgcn_perm(r2_15, r2_13, 0x07060302); SB();
  __builtin_amdgcn_s_waitcnt(0x477c);
  AMFMA(acc4_0, wf1_0, af1_4); SB();
  const u32 r5_4 = *(const u32*)(wb5 + voffA + 512); wf3_0[0] = __builtin_amdgcn_perm(r3_2, r3_0, 0x05040100); SB();
  AMFMA(acc4_1, wf1_1, af1_4); SB();
  const u32x4 af2_2 = *(LAS u32x4*)(ad0_2 + 2 * PLANE); const u32 r5_5 = *(const u32*)(wb5 + voffA + 576); SB();
  AMFMA(acc4_2, wf1_2, af1_4); SB();
  wf3_1[0] = __builtin_amdgcn_perm(r3_2, r3_0, 0x07060302); wf3_2[0] = __builtin_amdgcn_perm(r3_3, r3_1, 0x05040100); SB();
  AMFMA(acc4_3, wf1_3, af1_4); SB();
  const u32 r5_6 = *(const u32*)(wb5 + voffA + 768); wf3_3[0] = __builtin_amdgcn_perm(r3_3, r3_1, 0x07060302); SB();
  AMFMA(acc5_0, wf1_0, af1_5); SB();
  const u32 r5_7 = *(const u32*)(wb5 + voffA + 832); wf3_0[1] = __builtin_amdgcn_perm(r3_6, r3_4, 0x05040100); SB();
  AMFMA(acc5_1, wf1_1, af1_5); SB();
  const u32x4 af3_2 = *(LAS u32x4*)(ad0_2 + 3 * PLANE); const u32 r5_8 = *(const u32*)(wb5 + voffA + 1024); SB();
  AMFMA(acc5_2, wf1_2, af1_5); SB();
  wf3_1[1] = __builtin_amdgcn_perm(r3_6, r3_4, 0x07060302); wf3_2[1] = __builtin_amdgcn_perm(r3_7, r3_5, 0x05040100); SB();
  AMFMA(acc5_3, wf1_3, af1_5); SB();
  const u32 r5_9 = *(const u32*)(wb5 + voffA + 1088); wf3_3[1] = __builtin_amdgcn_perm(r3_7, r3_5, 0x07060302); SB();
  __builtin_amdgcn_s_waitcnt(0x467a);
  AMFMA(acc6_0, wf1_0, af1_6); SB();
  const u32 r5_10 = *(const u32*)(wb5 + voffA + 1280); wf3_0[2] = __builtin_amdgcn_perm(r3_10, r3_8, 0x05040100); SB();
  AMFMA(acc6_1, wf1_1, af1_6); SB();
  const u32x4 af2_3 = *(LAS u32x4*)(ad0_3 + 2 * PLANE); const u32 r5_11 = *(const u32*)(wb5 + voffA + 1344); SB();
  AMFMA(acc6_2, wf1_2, af1_6); SB();
  wf3_1[2] = __builtin_amdgcn_perm(r3_10, r3_8, 0x07060302); wf3_2[2] = __builtin_amdgcn_perm(r3_11, r3_9, 0x05040100); SB();
  AMFMA(acc6_3, wf1_3, af1_6); SB();
  const u32 r5_12 = *(const u32*)(wb5 + voffA + 1536); wf3_3[2] = __builtin_amdgcn_perm(r3_11, r3_9, 0x07060302); SB();
  VMFMA(acc7_0, wf1_0, af1_7); SB();
  const u32 r5_13 = *(const u32*)(wb5 + voffA + 1600); wf3_0[3] = __builtin_amdgcn_perm(r3_14, r3_12, 0x05040100); SB();
  VMFMA(acc7_1, wf1_1, af1_7); SB();
  const u32x4 af3_3 = *(LAS u32x4*)(ad0_3 + 3 * PLANE); const u32 r5_14 = *(const u32*)(wb5 + voffA + 1792); SB();
  VMFMA(acc7_2, wf1_2, af1_7); SB();
  wf3_1[3] = __builtin_amdgcn_perm(r3_14, r3_12, 0x07060302); wf3_2[3] = __builtin_amdgcn_perm(r3_15, r3_13, 0x05040100); SB();
  VMFMA(acc7_3, wf1_3, af1_7); SB();
  const u32 r5_15 = *(const u32*)(wb5 + voffA + 1856); wf3_3[3] = __builtin_amdgcn_perm(r3_15, r3_13, 0x07060302); SB();
  __builtin_amdgcn_s_waitcnt(0x467c);
  AMFMA(acc0_0, wf2_0, af2_0); SB();
  u32 wo6 = moff2 + 8192; SPIN(wo6); const char* wb6 = W1 + wo6; const u32 r6_0 = *(const u32*)(wb6 + voffA + 0); const u32x4 iv4_4 = *(volatile LAS u32x4*)(ja0 + 25600); SB();
  AMFMA(acc0_0, wf3_0, af3_0); SB();
  const u32x4 af2_4 = *(LAS u32x4*)(ad0_4 + 2 * PLANE); const u32 r6_1 = *(const u32*)(wb6 + voffA + 64); SB();
  AMFMA(acc0_1, wf2_1, af2_0); SB();
  wf4_0[0] = __builtin_amdgcn_perm(r4_2, r4_0, 0x05040100); wf4_1[0] = __builtin_amdgcn_perm(r4_2, r4_0, 0x07060302); SB();
  AMFMA(acc0_1, wf3_1, af3_0); SB();
  const u32 r6_2 = *(const u32*)(wb6 + voffA + 256); SB();
  AMFMA(acc0_2, wf2_2, af2_0); SB();
  const u32 r6_3 = *(const u32*)(wb6 + voffA + 320); SB();
  AMFMA(acc0_2, wf3_2, af3_0); SB();
  const u32x4 af3_4 = *(LAS u32x4*)(ad0_4 + 3 * PLANE); const u32 r6_4 = *(const u32*)(wb6 + voffA + 512); SB();
  AMFMA(acc0_3, wf2_3, af2_0); SB();
  wf4_2[0] = __builtin_amdgcn_perm(r4_3, r4_1, 0x05040100); wf4_3[0] = __builtin_amdgcn_perm(r4_3, r4_1, 0x07060302); SB();
  AMFMA(acc0_3, wf3_3, af3_0); SB();
  const u32 r6_5 = *(const u32*)(wb6 + voffA + 576); SB();
  __builtin_amdgcn_s_waitcnt(0x477e);
  AMFMA(acc1_0, wf2_0, af2_1); SB();
  const u32 r6_6 = *(const u32*)(wb6 + voffA + 768); const u32x4 iv4_5 = *(volatile LAS u32x4*)(ja1 + 25600); SB();
  AMFMA(acc1_0, wf3_0, af3_1); SB();
  const u32x4 af2_5 = *(LAS u32x4*)(ad0_5 + 2 * PLANE); const u32 r6_7 = *(const u32*)(wb6 + voffA + 832); SB();
  AMFMA(acc1_1, wf2_1, af2_1); SB();
  wf4_0[1] = __builtin_amdgcn_perm(r4_6, r4_4, 0x05040100); wf4_1[1] = __builtin_amdgcn_perm(r4_6, r4_4, 0x07060302); SB();
  AMFMA(acc1_1, wf3_1, af3_1); SB();
  const u32 r6_8 = *(const u32*)(wb6 + voffA + 1024); SB();
  AMFMA(acc1_2, wf2_2, af2_1); SB();
  const u32 r6_9 = *(const u32*)(wb6 + voffA + 1088); SB();
  AMFMA(acc1_2, wf3_2, af3_1); SB();
  const u32x4 af3_5 = *(LAS u32x4*)(ad0_5 + 3 * PLANE); const u32 r6_10 = *(const u32*)(wb6 + voffA + 1280); SB();
  AMFMA(acc1_3, wf2_3, af2_1); SB();
  wf4_2[1] = __builtin_amdgcn_perm(r4_7, r4_5, 0x05040100); wf4_3[1] = __builtin_amdgcn_perm(r4_7, r4_5, 0x07060302); SB();
  AMFMA(acc1_3, wf3_3, af3_1); SB();
  const u32 r6_11 = *(const u32*)(wb6 + voffA + 1344); SB();
  __builtin_amdgcn_s_waitcnt(0x8870);
  AMFMA(acc2_0, wf2_0, af2_2); SB();
  const u32 r6_12 = *(const u32*)(wb6 + voffA + 1536); const u32x4 iv4_6 = *(volatile LAS u32x4*)(ja2 + 25600); SB();
  AMFMA(acc2_0, wf3_0, af3_2); SB();
  const u32x4 af2_6 = *(LAS u32x4*)(ad0_6 + 2 * PLANE); const u32 r6_13 = *(const u32*)(wb6 + voffA + 1600); SB();
  AMFMA(acc2_1, wf2_1, af2_2); SB();
  wf4_0[2] = __builtin_amdgcn_perm(r4_10, r4_8, 0x05040100); wf4_1[2] = __builtin_amdgcn_perm(r4_10, r4_8, 0x07060302); SB();
  AMFMA(acc2_1, wf3_1, af3_2); SB();
  const u32 r6_14 = *(const u32*)(wb6 + voffA + 1792); SB();
  AMFMA(acc2_2, wf2_2, af2_2); SB();
  const u32 r6_15 = *(const u32*)(wb6 + voffA + 1856); SB();
  AMFMA(acc2_2, wf3_2, af3_2); SB();
  const u32x4 af3_6 = *(LAS u32x4*)(ad0_6 + 3 * PLANE); u32 wo7 = moff3 + 8192; SPIN(wo7); const char* wb7 = W1 + wo7; const u32 r7_0 = *(const u32*)(wb7 + voffA + 0); SB();
  AMFMA(acc2_3, wf2_3, af2_2); SB();
  wf4_2[2] = __builtin_amdgcn_perm(r4_11, r4_9, 0x05040100); wf4_3[2] = __builtin_amdgcn_perm(r4_11, r4_9, 0x07060302); SB();
  AMFMA(acc2_3, wf3_3, af3_2); SB();
  const u32 r7_1 = *(const u32*)(wb7 + voffA + 64); SB();
  __builtin_amdgcn_s_waitcnt(0x8972);
  VMFMA(acc3_0, wf2_0, af2_3); SB();
  const u32 r7_2 = *(const u32*)(wb7 + voffA + 256); const u32x4 iv4_7 = *(volatile LAS u32x4*)(ja3 + 25600); SB();
  VMFMA(acc3_0, wf3_0, af3_3); SB();
  const u32x4 af2_7 = *(LAS u32x4*)(ad0_7 + 2 * PLANE); const u32 r7_3 = *(const u32*)(wb7 + voffA + 320); SB();
  VMFMA(acc3_1, wf2_1, af2_3); SB();
  wf4_0[3] = __builtin_amdgcn_perm(r4_14, r4_12, 0x05040100); wf4_1[3] = __builtin_amdgcn_perm(r4_14, r4_12, 0x07060302); SB();
  VMFMA(acc3_1, wf3_1, af3_3); SB();
  const u32 r7_4 = *(const u32*)(wb7 + voffA + 512); u32 ad1_0 = (iv4_0[1] << 4) + lb; PIN(ad1_0); asm volatile("" :: "v"(iv4_0[0])); SB();
  VMFMA(acc3_2, wf2_2, af2_3); SB();
  const u32 r7_5 = *(const u32*)(wb7 + voffA + 576); SB();
  VMFMA(acc3_2, wf3_2, af3_3); SB();
  const u32x4 af3_7 = *(LAS u32x4*)(ad0_7 + 3 * PLANE); const u32 r7_6 = *(const u32*)(wb7 + voffA + 768); SB();
  VMFMA(acc3_3, wf2_3, af2_3); SB();
  wf4_2[3] = __builtin_amdgcn_perm(r4_15, r4_13, 0x05040100); wf4_3[3] = __builtin_amdgcn_perm(r4_15, r4_13, 0x07060302); SB();
  VMFMA(acc3_3, wf3_3, af3_3); SB();
  const u32 r7_7 = *(const u32*)(wb7 + voffA + 832); SB();
  __builtin_amdgcn_s_waitcnt(0x8974);
  AMFMA(acc4_0, wf2_0, af2_4); SB();
  const u32 r7_8 = *(const u32*)(wb7 + voffA + 1024); SB();
  AMFMA(acc4_0, wf3_0, af3_4); SB();
  const u32x4 af4_0 = *(LAS u32x4*)(ad1_0 + 0 * PLANE); const u32 r7_9 = *(const u32*)(wb7 + voffA + 1088); SB();
  AMFMA(acc4_1, wf2_1, af2_4); SB();
  wf5_0[0] = __builtin_amdgcn_perm(r5_2, r5_0, 0x05040100); wf5_1[0] = __builtin_amdgcn_perm(r5_2, r5_0, 0x07060302); SB();
  AMFMA(acc4_1, wf3_1, af3_4); SB();
  const u32 r7_10 = *(const u32*)(wb7 + voffA + 1280); u32 ad1_1 = (iv4_1[1] << 4) + lb; PIN(ad1_1); asm volatile("" :: "v"(iv4_1[0])); SB();
  AMFMA(acc4_2, wf2_2, af2_4); SB();
  const u32 r7_11 = *(const u32*)(wb7 + voffA + 1344); SB();
  AMFMA(acc4_2, wf3_2, af3_4); SB();
  const u32x4 af5_0 = *(LAS u32x4*)(ad1_0 + 1 * PLANE); const u32 r7_12 = *(const u32*)(wb7 + voffA + 1536); SB();
  AMFMA(acc4_3, wf2_3, af2_4); SB();
  wf5_2[0] = __builtin_amdgcn_perm(r5_3, r5_1, 0x05040100); wf5_3[0] = __builtin_amdgcn_perm(r5_3, r5_1, 0x07060302); SB();
  AMFMA(acc4_3, wf3_3, af3_4); SB();
  const u32 r7_13 = *(const u32*)(wb7 + voffA + 1600); SB();
  __builtin_amdgcn_s_waitcnt(0x8876);
  AMFMA(acc5_0, wf2_0, af2_5); SB();
  const u32 r7_14 = *(const u32*)(wb7 + voffA + 1792); SB();
  AMFMA(acc5_0, wf3_0, af3_5); SB();
  const u32x4 af4_1 = *(LAS u32x4*)(ad1_1 + 0 * PLANE); const u32 r7_15 = *(const u32*)(wb7 + voffA + 1856); SB();
  AMFMA(acc5_1, wf2_1, af2_5); SB();
  wf5_0[1] = __builtin_amdgcn_perm(r5_6, r5_4, 0x05040100); wf5_1[1] = __builtin_amdgcn_perm(r5_6, r5_4, 0x07060302); SB();
  AMFMA(acc5_1, wf3_1, af3_5); SB();
  u32 wo8 = moff0 + 16384; SPIN(wo8); const char* wb8 = W1 + wo8; const u32 r8_0 = *(const u32*)(wb8 + voffA + 0); u32 ad1_2 = (iv4_2[1] << 4) + lb; PIN(ad1_2); asm volatile("" :: "v"(iv4_2[0])); SB();
  AMFMA(acc5_2, wf2_2, af2_5); SB();
  const u32 r8_1 = *(const u32*)(wb8 + voffA + 64); SB();
  AMFMA(acc5_2, wf3_2, af3_5); SB();
  const u32x4 af5_1 = *(LAS u32x4*)(ad1_1 + 1 * PLANE); const u32 r8_2 = *(const u32*)(wb8 + voffA + 256); SB();
  AMFMA(acc5_3, wf2_3, af2_5); SB();
  wf5_2[1] = __builtin_amdgcn_perm(r5_7, r5_5, 0x05040100); wf5_3[1] = __builtin_amdgcn_perm(r5_7, r5_5, 0x07060302); SB();
  AMFMA(acc5_3, wf3_3, af3_5); SB();
  const u32 r8_3 = *(const u32*)(wb8 + voffA + 320); SB();
  __builtin_amdgcn_s_waitcnt(0x8775);
  AMFMA(acc6_0, wf2_0, af2_6); SB();
  const u32 r8_4 = *(const u32*)(wb8 + voffA + 512); wf5_0[2] = __builtin_amdgcn_perm(r5_10, r5_8, 0x05040100); SB();
  AMFMA(acc6_0, wf3_0, af3_6); SB();
  const u32x4 af4_2 = *(LAS u32x4*)(ad1_2 + 0 * PLANE); const u32 r8_5 = *(const u32*)(wb8 + voffA + 576); SB();
  AMFMA(acc6_1, wf2_1, af2_6); SB();
  wf5_1[2] = __builtin_amdgcn_perm(r5_10, r5_8, 0x07060302); wf5_2[2] = __builtin_amdgcn_perm(r5_11, r5_9, 0x05040100); SB();
  AMFMA(acc6_1, wf3_1, af3_6); SB();
  const u32 r8_6 = *(const u32*)(wb8 + voffA + 768); u32 ad1_3 = (iv4_3[1] << 4) + lb; PIN(ad1_3); asm volatile("" :: "v"(iv4_3[0])); SB();
  AMFMA(acc6_2, wf2_2, af2_6); SB();
  const u32 r8_7 = *(const u32*)(wb8 + voffA + 832); SB();
  AMFMA(acc6_2, wf3_2, af3_6); SB();
  const u32x4 af5_2 = *(LAS u32x4*)(ad1_2 + 1 * PLANE); const u32 r8_8 = *(const u32*)(wb8 + voffA + 1024); SB();
  AMFMA(acc6_3, wf2_3, af2_6); SB();
  wf5_3[2] = __builtin_amdgcn_perm(r5_11, r5_9, 0x07060302); wf5_0[3] = __builtin_amdgcn_perm(r5_14, r5_12, 0x05040100); SB();
  AMFMA(acc6_3, wf3_3, af3_6); SB();
  const u32 r8_9 = *(const u32*)(wb8 + voffA + 1088); SB();
  __builtin_amdgcn_s_waitcnt(0x8677);
  VMFMA(acc7_0, wf2_0, af2_7); SB();
  const u32 r8_10 = *(const u32*)(wb8 + voffA + 1280); u32 ad1_6 = (iv4_6[1] << 4) + lb; PIN(ad1_6); asm volatile("" :: "v"(iv4_6[0])); SB();
  VMFMA(acc7_0, wf3_0, af3_7); SB();
  const u32x4 af4_3 = *(LAS u32x4*)(ad1_3 + 0 * PLANE); const u32 r8_11 = *(const u32*)(wb8 + voffA + 1344); SB();
  VMFMA(acc7_1, wf2_1, af2_7); SB();
  wf5_1[3] = __builtin_amdgcn_perm(r5_14, r5_12, 0x07060302); wf5_2[3] = __builtin_amdgcn_perm(r5_15, r5_13, 0x05040100); SB();
  VMFMA(acc7_1, wf3_1, af3_7); SB();
  const u32 r8_12 = *(const u32*)(wb8 + voffA + 1536); u32 ad1_4 = (iv4_4[1] << 4) + lb; PIN(ad1_4); asm volatile("" :: "v"(iv4_4[0])); SB();
  VMFMA(acc7_2, wf2_2, af2_7); SB();
  const u32 r8_13 = *(const u32*)(wb8 + voffA + 1600); wf5_3[3] = __builtin_amdgcn_perm(r5_15, r5_13, 0x07060302); SB();
  VMFMA(acc7_2, wf3_2, af3_7); SB();
  const u32x4 af5_3 = *(LAS u32x4*)(ad1_3 + 1 * PLANE); const u32 r8_14 = *(const u32*)(wb8 + voffA + 1792); SB();
  VMFMA(acc7_3, wf2_3, af2_7); SB();
  wf6_0[0] = __builtin_amdgcn_perm(r6_2, r6_0, 0x05040100); wf6_1[0] = __builtin_amdgcn_perm(r6_2, r6_0, 0x07060302); SB();
  VMFMA(acc7_3, wf3_3, af3_7); SB();
  const u32 r8_15 = *(const u32*)(wb8 + voffA + 1856); u32 ad1_5 = (iv4_5[1] << 4) + lb; PIN(ad1_5); asm volatile("" :: "v"(iv4_5[0])); SB();
  __builtin_amdgcn_s_waitcnt(0x8679);
  AMFMA(acc0_0, wf4_0, af4_0); SB();
  u32 wo9 = moff1 + 16384; SPIN(wo9); const char* wb9 = W1 + wo9; const u32 r9_0 = *(const u32*)(wb9 + voffA + 0); u32 jb0 = ja0 + d2; PIN(jb0); SB();
  AMFMA(acc0_0, wf5_0, af5_0); SB();
  const u32x4 af4_4 = *(LAS u32x4*)(ad1_4 + 0 * PLANE); const u32 r9_1 = *(const u32*)(wb9 + voffA + 64); SB();
  AMFMA(acc0_1, wf4_1, af4_0); SB();
  wf6_2[0] = __builtin_amdgcn_perm(r6_3, r6_1, 0x05040100); wf6_3[0] = __builtin_amdgcn_perm(r6_3, r6_1, 0x07060302); SB();
  AMFMA(acc0_1, wf5_1, af5_0); SB();
  const u32 r9_2 = *(const u32*)(wb9 + voffA + 256); u32 jc0 = ja0 + d1; PIN(jc0); SB();
  AMFMA(acc0_2, wf4_2, af4_0); SB();
  const u32 r9_3 = *(const u32*)(wb9 + voffA + 320); u32 jb1 = ja1 + d2; PIN(jb1); SB();
  AMFMA(acc0_2, wf5_2, af5_0); SB();
  const u32x4 af5_4 = *(LAS u32x4*)(ad1_4 + 1 * PLANE); u32 jc1 = ja1 + d1; PIN(jc1); SB();
  AMFMA(acc0_3, wf4_3, af4_0); SB();
  wf6_0[1] = __builtin_amdgcn_perm(r6_6, r6_4, 0x05040100); wf6_1[1] = __builtin_amdgcn_perm(r6_6, r6_4, 0x07060302); SB();
  AMFMA(acc0_3, wf5_3, af5_0); SB();
  const u32 r9_4 = *(const u32*)(wb9 + voffA + 512); u32 jb2 = ja2 + d2; PIN(jb2); SB();
  __builtin_amdgcn_s_waitcnt(0x8679);
  AMFMA(acc1_0, wf4_0, af4_1); SB();
  const u32 r9_5 = *(const u32*)(wb9 + voffA + 576); wf6_2[1] = __builtin_amdgcn_perm(r6_7, r6_5, 0x05040100); SB();
  AMFMA(acc1_0, wf5_0, af5_1); SB();
  const u32x4 af4_5 = *(LAS u32x4*)(ad1_5 + 0 * PLANE); u32 jc2 = ja2 + d1; PIN(jc2); SB();
  AMFMA(acc1_1, wf4_1, af4_1); SB();
  wf6_3[1] = __builtin_amdgcn_perm(r6_7, r6_5, 0x07060302); wf6_0[2] = __builtin_amdgcn_perm(r6_10, r6_8, 0x05040100); SB();
  AMFMA(acc1_1, wf5_1, af5_1); SB();
  const u32 r9_6 = *(const u32*)(wb9 + voffA + 768); u32 jb3 = ja3 + d2; PIN(jb3); SB();
  AMFMA(acc1_2, wf4_2, af4_1); SB();
  const u32 r9_7 = *(const u32*)(wb9 + voffA + 832); u32 jc3 = ja3 + d1; PIN(jc3); SB();
  AMFMA(acc1_2, wf5_2, af5_1); SB();
  const u32x4 af5_5 = *(LAS u32x4*)(ad1_5 + 1 * PLANE); SB();
  AMFMA(acc1_3, wf4_3, af4_1); SB();
  wf6_1[2] = __builtin_amdgcn_perm(r6_10, r6_8, 0x07060302); wf6_2[2] = __builtin_amdgcn_perm(r6_11, r6_9, 0x05040100); SB();
  AMFMA(acc1_3, wf5_3, af5_1); SB();
  const u32 r9_8 = *(const u32*)(wb9 + voffA + 1024); SB();
  __builtin_amdgcn_s_waitcnt(0x8679);
  AMFMA(acc2_0, wf4_0, af4_2); SB();
  const u32 r9_9 = *(const u32*)(wb9 + voffA + 1088); u32 ad1_7 = (iv4_7[1] << 4) + lb; PIN(ad1_7); asm volatile("" :: "v"(iv4_7[0])); SB();
  AMFMA(acc2_0, wf5_0, af5_2); SB();
  const u32x4 af4_6 = *(LAS u32x4*)(ad1_6 + 0 * PLANE); const u32 r9_10 = *(const u32*)(wb9 + voffA + 1280); SB();
  AMFMA(acc2_1, wf4_1, af4_2); SB();
  wf6_3[2] = __builtin_amdgcn_perm(r6_11, r6_9, 0x07060302); wf6_0[3] = __builtin_amdgcn_perm(r6_14, r6_12, 0x05040100); SB();
  AMFMA(acc2_1, wf5_1, af5_2); SB();
  const u32 r9_11 = *(const u32*)(wb9 + voffA + 1344); const u32x2 iv2_0 = *(volatile LAS u32x2*)(jb0 + 0); SB();
  AMFMA(acc2_2, wf4_2, af4_2); SB();
  const u32 r9_12 = *(const u32*)(wb9 + voffA + 1536); SB();
  AMFMA(acc2_2, wf5_2, af5_2); SB();
  const u32x4 af5_6 = *(LAS u32x4*)(ad1_6 + 1 * PLANE); SB();
  AMFMA(acc2_3, wf4_3, af4_2); SB();
  wf6_1[3] = __builtin_amdgcn_perm(r6_14, r6_12, 0x07060302); wf6_2[3] = __builtin_amdgcn_perm(r6_15, r6_13, 0x05040100); SB();
  AMFMA(acc2_3, wf5_3, af5_2); SB();
  const u32 r9_13 = *(const u32*)(wb9 + voffA + 1600); SB();
  __builtin_amdgcn_s_waitcnt(0x877a);
  VMFMA(acc3_0, wf4_0, af4_3); SB();
  const u32 r9_14 = *(const u32*)(wb9 + voffA + 1792); wf6_3[3] = __builtin_amdgcn_perm(r6_15, r6_13, 0x07060302); SB();
  VMFMA(acc3_0, wf5_0, af5_3); SB();
  const u32x4 af4_7 = *(LAS u32x4*)(ad1_7 + 0 * PLANE); SB();
  VMFMA(acc3_1, wf4_1, af4_3); SB();
  wf7_0[0] = __builtin_amdgcn_perm(r7_2, r7_0, 0x05040100); wf7_1[0] = __builtin_amdgcn_perm(r7_2, r7_0, 0x07060302); SB();
  VMFMA(acc3_1, wf5_1, af5_3); SB();
  const u32 r9_15 = *(const u32*)(wb9 + voffA + 1856); const u32 ive0 = *(volatile LAS u32*)(jc0 + 0); SB();
  VMFMA(acc3_2, wf4_2, af4_3); SB();
  u32 wo10 = moff2 + 16384; SPIN(wo10); const char* wb10 = W1 + wo10; const u32 r10_0 = *(const u32*)(wb10 + voffA + 0); SB();
  VMFMA(acc3_2, wf5_2, af5_3); SB();
  const u32x4 af5_7 = *(LAS u32x4*)(ad1_7 + 1 * PLANE); const u32 r10_1 = *(const u32*)(wb10 + voffA + 64); SB();
  VMFMA(acc3_3, wf4_3, af4_3); SB();
  wf7_2[0] = __builtin_amdgcn_perm(r7_3, r7_1, 0x05040100); wf7_3[0] = __builtin_amdgcn_perm(r7_3, r7_1, 0x07060302); SB();
  VMFMA(acc3_3, wf5_3, af5_3); SB();
  const u32 r10_2 = *(const u32*)(wb10 + voffA + 256); SB();
  __builtin_amdgcn_s_waitcnt(0x8878);
  AMFMA(acc4_0, wf4_0, af4_4); SB();
  const u32 r10_3 = *(const u32*)(wb10 + voffA + 320); SB();
  AMFMA(acc4_0, wf5_0, af5_4); SB();
  const u32x4 af6_0 = *(LAS u32x4*)(ad1_0 + 2 * PLANE); SB();
  AMFMA(acc4_1, wf4_1, af4_4); SB();
  wf7_0[1] = __builtin_amdgcn_perm(r7_6, r7_4, 0x05040100); wf7_1[1] = __builtin_amdgcn_perm(r7_6, r7_4, 0x07060302); SB();
  AMFMA(acc4_1, wf5_1, af5_4); SB();
  const u32 r10_4 = *(const u32*)(wb10 + voffA + 512); const u32x2 iv2_1 = *(volatile LAS u32x2*)(jb1 + 0); SB();
  AMFMA(acc4_2, wf4_2, af4_4); SB();
  const u32 r10_5 = *(const u32*)(wb10 + voffA + 576); wf7_2[1] = __builtin_amdgcn_perm(r7_7, r7_5, 0x05040100); SB();
  AMFMA(acc4_2, wf5_2, af5_4); SB();
  const u32x4 af7_0 = *(LAS u32x4*)(ad1_0 + 3 * PLANE); SB();
  AMFMA(acc4_3, wf4_3, af4_4); SB();
  wf7_3[1] = __builtin_amdgcn_perm(r7_7, r7_5, 0x07060302); wf7_0[2] = __builtin_amdgcn_perm(r7_10, r7_8, 0x05040100); SB();
  AMFMA(acc4_3, wf5_3, af5_4); SB();
  const u32 r10_6 = *(const u32*)(wb10 + voffA + 768); SB();
  __builtin_amdgcn_s_waitcnt(0x8978);
  AMFMA(acc5_0, wf4_0, af4_5); SB();
  const u32 r10_7 = *(const u32*)(wb10 + voffA + 832); SB();
  AMFMA(acc5_0, wf5_0, af5_5); SB();
  const u32x4 af6_1 = *(LAS u32x4*)(ad1_1 + 2 * PLANE); const u32 r10_8 = *(const u32*)(wb10 + voffA + 1024); SB();
  AMFMA(acc5_1, wf4_1, af4_5); SB();
  wf7_1[2] = __builtin_amdgcn_perm(r7_10, r7_8, 0x07060302); wf7_2[2] = __builtin_amdgcn_perm(r7_11, r7_9, 0x05040100); SB();
  AMFMA(acc5_1, wf5_1, af5_5); SB();
  const u32 r10_9 = *(const u32*)(wb10 + voffA + 1088); const u32 ive1 = *(volatile LAS u32*)(jc1 + 0); SB();
  AMFMA(acc5_2, wf4_2, af4_5); SB();
  const u32 r10_10 = *(const u32*)(wb10 + voffA + 1280); SB();
  AMFMA(acc5_2, wf5_2, af5_5); SB();
  const u32x4 af7_1 = *(LAS u32x4*)(ad1_1 + 3 * PLANE); SB();
  AMFMA(acc5_3, wf4_3, af4_5); SB();
  wf7_3[2] = __builtin_amdgcn_perm(r7_11, r7_9, 0x07060302); wf7_0[3] = __builtin_amdgcn_perm(r7_14, r7_12, 0x05040100); SB();
  AMFMA(acc5_3, wf5_3, af5_5); SB();
  const u32 r10_11 = *(const u32*)(wb10 + voffA + 1344); SB();
  __builtin_amdgcn_s_waitcnt(0x8979);
  AMFMA(acc6_0, wf4_0, af4_6); SB();
  const u32 r10_12 = *(const u32*)(wb10 + voffA + 1536); wf7_1[3] = __builtin_amdgcn_perm(r7_14, r7_12, 0x07060302); SB();
  AMFMA(acc6_0, wf5_0, af5_6); SB();
  const u32x4 af6_2 = *(LAS u32x4*)(ad1_2 + 2 * PLANE); SB();
  AMFMA(acc6_1, wf4_1, af4_6); SB();
  wf7_2[3] = __builtin_amdgcn_perm(r7_15, r7_13, 0x05040100); wf7_3[3] = __builtin_amdgcn_perm(r7_15, r7_13, 0x07060302); SB();
  AMFMA(acc6_1, wf5_1, af5_6); SB();
  const u32 r10_13 = *(const u32*)(wb10 + voffA + 1600); const u32x2 iv2_2 = *(volatile LAS u32x2*)(jb2 + 0); SB();
  AMFMA(acc6_2, wf4_2, af4_6); SB();
  const u32 r10_14 = *(const u32*)(wb10 + voffA + 1792); SB();
  AMFMA(acc6_2, wf5_2, af5_6); SB();
  const u32x4 af7_2 = *(LAS u32x4*)(ad1_2 + 3 * PLANE); const u32 r10_15 = *(const u32*)(wb10 + voffA + 1856); SB();
  AMFMA(acc6_3, wf4_3, af4_6); SB();
  wf8_0[0] = __builtin_amdgcn_perm(r8_2, r8_0, 0x05040100); wf8_1[0] = __builtin_amdgcn_perm(r8_2, r8_0, 0x07060302); SB();
  AMFMA(acc6_3, wf5_3, af5_6); SB();
  u32 wo11 = moff3 + 16384; SPIN(wo11); const char* wb11 = W1 + wo11; const u32 r11_0 = *(const u32*)(wb11 + voffA + 0); SB();
  __builtin_amdgcn_s_waitcnt(0x8979);
  VMFMA(acc7_0, wf4_0, af4_7); SB();
  const u32 r11_1 = *(const u32*)(wb11 + voffA + 64); SB();
  VMFMA(acc7_0, wf5_0, af5_7); SB();
  const u32x4 af6_3 = *(LAS u32x4*)(ad1_3 + 2 * PLANE); SB();
  VMFMA(acc7_1, wf4_1, af4_7); SB();
  wf8_2[0] = __builtin_amdgcn_perm(r8_3, r8_1, 0x05040100); wf8_3[0] = __builtin_amdgcn_perm(r8_3, r8_1, 0x07060302); SB();
  VMFMA(acc7_1, wf5_1, af5_7); SB();
  const u32 r11_2 = *(const u32*)(wb11 + voffA + 256); const u32 ive2 = *(volatile LAS u32*)(jc2 + 0); SB();
  VMFMA(acc7_2, wf4_2, af4_7); SB();
  const u32 r11_3 = *(const u32*)(wb11 + voffA + 320); wf8_0[1] = __builtin_amdgcn_perm(r8_6, r8_4, 0x05040100); SB();
  VMFMA(acc7_2, wf5_2, af5_7); SB();
  const u32x4 af7_3 = *(LAS u32x4*)(ad1_3 + 3 * PLANE); SB();
  VMFMA(acc7_3, wf4_3, af4_7); SB();
  wf8_1[1] = __builtin_amdgcn_perm(r8_6, r8_4, 0x07060302); wf8_2[1] = __builtin_amdgcn_perm(r8_7, r8_5, 0x05040100); SB();
  VMFMA(acc7_3, wf5_3, af5_7); SB();
  const u32 r11_4 = *(const u32*)(wb11 + voffA + 512); SB();
  __builtin_amdgcn_s_waitcnt(0x8979);
  AMFMA(acc0_0, wf6_0, af6_0); SB();
  const u32 r11_5 = *(const u32*)(wb11 + voffA + 576); SB();
  AMFMA(acc0_0, wf7_0, af7_0); SB();
  const u32x4 af6_4 = *(LAS u32x4*)(ad1_4 + 2 * PLANE); const u32 r11_6 = *(const u32*)(wb11 + voffA + 768); SB();
  AMFMA(acc0_1, wf6_1, af6_0); SB();
  wf8_3[1] = __builtin_amdgcn_perm(r8_7, r8_5, 0x07060302); wf8_0[2] = __builtin_amdgcn_perm(r8_10, r8_8, 0x05040100); SB();
  AMFMA(acc0_1, wf7_1, af7_0); SB();
  const u32 r11_7 = *(const u32*)(wb11 + voffA + 832); const u32x2 iv2_3 = *(volatile LAS u32x2*)(jb3 + 0); SB();
  AMFMA(acc0_2, wf6_2, af6_0); SB();
  const u32 r11_8 = *(const u32*)(wb11 + voffA + 1024); SB();
  AMFMA(acc0_2, wf7_2, af7_0); SB();
  const u32x4 af7_4 = *(LAS u32x4*)(ad1_4 + 3 * PLANE); SB();
  AMFMA(acc0_3, wf6_3, af6_0); SB();
  wf8_1[2] = __builtin_amdgcn_perm(r8_10, r8_8, 0x07060302); wf8_2[2] = __builtin_amdgcn_perm(r8_11, r8_9, 0x05040100); SB();
  AMFMA(acc0_3, wf7_3, af7_0); SB();
  const u32 r11_9 = *(const u32*)(wb11 + voffA + 1088); SB();
  __builtin_amdgcn_s_waitcnt(0x897a);
  AMFMA(acc1_0, wf6_0, af6_1); SB();
  const u32 r11_10 = *(const u32*)(wb11 + voffA + 1280); wf8_3[2] = __builtin_amdgcn_perm(r8_11, r8_9, 0x07060302); SB();
  AMFMA(acc1_0, wf7_0, af7_1); SB();
  const u32x4 af6_5 = *(LAS u32x4*)(ad1_5 + 2 * PLANE); SB();
  AMFMA(acc1_1, wf6_1, af6_1); SB();
  wf8_0[3] = __builtin_amdgcn_perm(r8_14, r8_12, 0x05040100); wf8_1[3] = __builtin_amdgcn_perm(r8_14, r8_12, 0x07060302); SB();
  AMFMA(acc1_1, wf7_1, af7_1); SB();
  const u32 r11_11 = *(const u32*)(wb11 + voffA + 1344); const u32 ive3 = *(volatile LAS u32*)(jc3 + 0); SB();
  AMFMA(acc1_2, wf6_2, af6_1); SB();
  const u32 r11_12 = *(const u32*)(wb11 + voffA + 1536); SB();
  AMFMA(acc1_2, wf7_2, af7_1); SB();
  const u32x4 af7_5 = *(LAS u32x4*)(ad1_5 + 3 * PLANE); SB();
  AMFMA(acc1_3, wf6_3, af6_1); SB();
  wf8_2[3] = __builtin_amdgcn_perm(r8_15, r8_13, 0x05040100); wf8_3[3] = __builtin_amdgcn_perm(r8_15, r8_13, 0x07060302); SB();
  AMFMA(acc1_3, wf7_3, af7_1); SB();
  const u32 r11_13 = *(const u32*)(wb11 + voffA + 1600); SB();
  __builtin_amdgcn_s_waitcnt(0x897a);
  AMFMA(acc2_0, wf6_0, af6_2); SB();
  const u32 r11_14 = *(const u32*)(wb11 + voffA + 1792); SB();
  AMFMA(acc2_0, wf7_0, af7_2); SB();
  const u32x4 af6_6 = *(LAS u32x4*)(ad1_6 + 2 * PLANE); const u32 r11_15 = *(const u32*)(wb11 + voffA + 1856); SB();
  AMFMA(acc2_1, wf6_1, af6_2); SB();
  wf9_0[0] = __builtin_amdgcn_perm(r9_2, r9_0, 0x05040100); wf9_1[0] = __builtin_amdgcn_perm(r9_2, r9_0, 0x07060302); SB();
  AMFMA(acc2_1, wf7_1, af7_2); SB();
  u32 wo12 = moff0 + 24576; SPIN(wo12); const char* wb12 = W1 + wo12; const u32 r12_0 = *(const u32*)(wb12 + voffA + 0); const u32x2 iv2_4 = *(volatile LAS u32x2*)(jb0 + 25600); SB();
  AMFMA(acc2_2, wf6_2, af6_2); SB();
  const u32 r12_1 = *(const u32*)(wb12 + voffA + 64); SB();
  AMFMA(acc2_2, wf7_2, af7_2); SB();
  const u32x4 af7_6 = *(LAS u32x4*)(ad1_6 + 3 * PLANE); SB();
  AMFMA(acc2_3, wf6_3, af6_2); SB();
  wf9_2[0] = __builtin_amdgcn_perm(r9_3, r9_1, 0x05040100); wf9_3[0] = __builtin_amdgcn_perm(r9_3, r9_1, 0x07060302); SB();
  AMFMA(acc2_3, wf7_3, af7_2); SB();
  const u32 r12_2 = *(const u32*)(wb12 + voffA + 256); SB();
  __builtin_amdgcn_s_waitcnt(0x8978);
  VMFMA(acc3_0, wf6_0, af6_3); SB();
  const u32 r12_3 = *(const u32*)(wb12 + voffA + 320); wf9_0[1] = __builtin_amdgcn_perm(r9_6, r9_4, 0x05040100); SB();
  VMFMA(acc3_0, wf7_0, af7_3); SB();
  const u32x4 af6_7 = *(LAS u32x4*)(ad1_7 + 2 * PLANE); u32 ad2_0 = (iv4_0[2] << 4) + lb; PIN(ad2_0); SB();
  VMFMA(acc3_1, wf6_1, af6_3); SB();
  wf9_1[1] = __builtin_amdgcn_perm(r9_6, r9_4, 0x07060302); wf9_2[1] = __builtin_amdgcn_perm(r9_7, r9_5, 0x05040100); SB();
  VMFMA(acc3_1, wf7_1, af7_3); SB();
  const u32 r12_4 = *(const u32*)(wb12 + voffA + 512); const u32 ive4 = *(volatile LAS u32*)(jc0 + 25600); SB();
  VMFMA(acc3_2, wf6_2, af6_3); SB();
  const u32 r12_5 = *(const u32*)(wb12 + voffA + 576); SB();
  VMFMA(acc3_2, wf7_2, af7_3); SB();
  const u32x4 af7_7 = *(LAS u32x4*)(ad1_7 + 3 * PLANE); const u32 r12_6 = *(const u32*)(wb12 + voffA + 768); SB();
  VMFMA(acc3_3, wf6_3, af6_3); SB();
  wf9_3[1] = __builtin_amdgcn_perm(r9_7, r9_5, 0x07060302); wf9_0[2] = __builtin_amdgcn_perm(r9_10, r9_8, 0x05040100); SB();
  VMFMA(acc3_3, wf7_3, af7_3); SB();
  const u32 r12_7 = *(const u32*)(wb12 + voffA + 832); SB();
  __builtin_amdgcn_s_waitcnt(0x8979);
  AMFMA(acc4_0, wf6_0, af6_4); SB();
  const u32 r12_8 = *(const u32*)(wb12 + voffA + 1024); SB();
  AMFMA(acc4_0, wf7_0, af7_4); SB();
  const u32x4 af8_0 = *(LAS u32x4*)(ad2_0 + 0 * PLANE); u32 ad2_1 = (iv4_1[2] << 4) + lb; PIN(ad2_1); SB();
  AMFMA(acc4_1, wf6_1, af6_4); SB();
  wf9_1[2] = __builtin_amdgcn_perm(r9_10, r9_8, 0x07060302); wf9_2[2] = __builtin_amdgcn_perm(r9_11, r9_9, 0x05040100); SB();
  AMFMA(acc4_1, wf7_1, af7_4); SB();
  const u32 r12_9 = *(const u32*)(wb12 + voffA + 1088); const u32x2 iv2_5 = *(volatile LAS u32x2*)(jb1 + 25600); SB();
  AMFMA(acc4_2, wf6_2, af6_4); SB();
  const u32 r12_10 = *(const u32*)(wb12 + voffA + 1280); wf9_3[2] = __builtin_amdgcn_perm(r9_11, r9_9, 0x07060302); SB();
  AMFMA(acc4_2, wf7_2, af7_4); SB();
  const u32x4 af9_0 = *(LAS u32x4*)(ad2_0 + 1 * PLANE); SB();
  AMFMA(acc4_3, wf6_3, af6_4); SB();
  wf9_0[3] = __builtin_amdgcn_perm(r9_14, r9_12, 0x05040100); wf9_1[3] = __builtin_amdgcn_perm(r9_14, r9_12, 0x07060302); SB();
  AMFMA(acc4_3, wf7_3, af7_4); SB();
  const u32 r12_11 = *(const u32*)(wb12 + voffA + 1344); SB();
  __builtin_amdgcn_s_waitcnt(0x8979);
  AMFMA(acc5_0, wf6_0, af6_5); SB();
  const u32 r12_12 = *(const u32*)(wb12 + voffA + 1536); u32 ad2_2 = (iv4_2[2] << 4) + lb; PIN(ad2_2); SB();
  AMFMA(acc5_0, wf7_0, af7_5); SB();
  const u32x4 af8_1 = *(LAS u32x4*)(ad2_1 + 0 * PLANE); const u32 r12_13 = *(const u32*)(wb12 + voffA + 1600); SB();
  AMFMA(acc5_1, wf6_1, af6_5); SB();
  wf9_2[3] = __builtin_amdgcn_perm(r9_15, r9_13, 0x05040100); wf9_3[3] = __builtin_amdgcn_perm(r9_15, r9_13, 0x07060302); SB();
  AMFMA(acc5_1, wf7_1, af7_5); SB();
  const u32 r12_14 = *(const u32*)(wb12 + voffA + 1792); const u32 ive5 = *(volatile LAS u32*)(jc1 + 25600); SB();
  AMFMA(acc5_2, wf6_2, af6_5); SB();
  const u32 r12_15 = *(const u32*)(wb12 + voffA + 1856); SB();
  AMFMA(acc5_2, wf7_2, af7_5); SB();
  const u32x4 af9_1 = *(LAS u32x4*)(ad2_1 + 1 * PLANE); SB();
  AMFMA(acc5_3, wf6_3, af6_5); SB();
  wf10_0[0] = __builtin_amdgcn_perm(r10_2, r10_0, 0x05040100); wf10_1[0] = __builtin_amdgcn_perm(r10_2, r10_0, 0x07060302); SB();
  AMFMA(acc5_3, wf7_3, af7_5); SB();
  u32 wo13 = moff1 + 24576; SPIN(wo13); const char* wb13 = W1 + wo13; const u32 r13_0 = *(const u32*)(wb13 + voffA + 0); SB();
  __builtin_amdgcn_s_waitcnt(0x8979);
  AMFMA(acc6_0, wf6_0, af6_6); SB();
  const u32 r13_1 = *(const u32*)(wb13 + voffA + 64); wf10_2[0] = __builtin_amdgcn_perm(r10_3, r10_1, 0x05040100); SB();
  AMFMA(acc6_0, wf7_0, af7_6); SB();
  const u32x4 af8_2 = *(LAS u32x4*)(ad2_2 + 0 * PLANE); u32 ad2_3 = (iv4_3[2] << 4) + lb; PIN(ad2_3); SB();
  AMFMA(acc6_1, wf6_1, af6_6); SB();
  wf10_3[0] = __builtin_amdgcn_perm(r10_3, r10_1, 0x07060302); wf10_0[1] = __builtin_amdgcn_perm(r10_6, r10_4, 0x05040100); SB();
  AMFMA(acc6_1, wf7_1, af7_6); SB();
  const u32 r13_2 = *(const u32*)(wb13 + voffA + 256); const u32x2 iv2_6 = *(volatile LAS u32x2*)(jb2 + 25600); SB();
  AMFMA(acc6_2, wf6_2, af6_6); SB();
  const u32 r13_3 = *(const u32*)(wb13 + voffA + 320); SB();
  AMFMA(acc6_2, wf7_2, af7_6); SB();
  const u32x4 af9_2 = *(LAS u32x4*)(ad2_2 + 1 * PLANE); const u32 r13_4 = *(const u32*)(wb13 + voffA + 512); SB();
  AMFMA(acc6_3, wf6_3, af6_6); SB();
  wf10_1[1] = __builtin_amdgcn_perm(r10_6, r10_4, 0x07060302); wf10_2[1] = __builtin_amdgcn_perm(r10_7, r10_5, 0x05040100); SB();
  AMFMA(acc6_3, wf7_3, af7_6); SB();
  const u32 r13_5 = *(const u32*)(wb13 + voffA + 576); SB();
  __builtin_amdgcn_s_waitcnt(0x897a);
  VMFMA(acc7_0, wf6_0, af6_7); SB();
  const u32 r13_6 = *(const u32*)(wb13 + voffA + 768); SB();
  VMFMA(acc7_0, wf7_0, af7_7); SB();
  const u32x4 af8_3 = *(LAS u32x4*)(ad2_3 + 0 * PLANE); u32 ad2_4 = (iv4_4[2] << 4) + lb; PIN(ad2_4); SB();
  VMFMA(acc7_1, wf6_1, af6_7); SB();
  wf10_3[1] = __builtin_amdgcn_perm(r10_7, r10_5, 0x07060302); wf10_0[2] = __builtin_amdgcn_perm(r10_10, r10_8, 0x05040100); SB();
  VMFMA(acc7_1, wf7_1, af7_7); SB();
  const u32 r13_7 = *(const u32*)(wb13 + voffA + 832); const u32 ive6 = *(volatile LAS u32*)(jc2 + 25600); SB();
  VMFMA(acc7_2, wf6_2, af6_7); SB();
  const u32 r13_8 = *(const u32*)(wb13 + voffA + 1024); wf10_1[2] = __builtin_amdgcn_perm(r10_10, r10_8, 0x07060302); SB();
  VMFMA(acc7_2, wf7_2, af7_7); SB();
  const u32x4 af9_3 = *(LAS u32x4*)(ad2_3 + 1 * PLANE); SB();
  VMFMA(acc7_3, wf6_3, af6_7); SB();
  wf10_2[2] = __builtin_amdgcn_perm(r10_11, r10_9, 0x05040100); wf10_3[2] = __builtin_amdgcn_perm(r10_11, r10_9, 0x07060302); SB();
  VMFMA(acc7_3, wf7_3, af7_7); SB();
  const u32 r13_9 = *(const u32*)(wb13 + voffA + 1088); SB();
  __builtin_amdgcn_s_waitcnt(0x897a);
  AMFMA(acc0_0, wf8_0, af8_0); SB();
  const u32 r13_10 = *(const u32*)(wb13 + voffA + 1280); u32 ad2_5 = (iv4_5[2] << 4) + lb; PIN(ad2_5); SB();
  AMFMA(acc0_0, wf9_0, af9_0); SB();
  const u32x4 af8_4 = *(LAS u32x4*)(ad2_4 + 0 * PLANE); const u32 r13_11 = *(const u32*)(wb13 + voffA + 1344); SB();
  AMFMA(acc0_1, wf8_1, af8_0); SB();
  wf10_0[3] = __builtin_amdgcn_perm(r10_14, r10_12, 0x05040100); wf10_1[3] = __builtin_amdgcn_perm(r10_14, r10_12, 0x07060302); SB();
  AMFMA(acc0_1, wf9_1, af9_0); SB();
  const u32 r13_12 = *(const u32*)(wb13 + voffA + 1536); const u32x2 iv2_7 = *(volatile LAS u32x2*)(jb3 + 25600); SB();
  AMFMA(acc0_2, wf8_2, af8_0); SB();
  const u32 r13_13 = *(const u32*)(wb13 + voffA + 1600); SB();
  AMFMA(acc0_2, wf9_2, af9_0); SB();
  const u32x4 af9_4 = *(LAS u32x4*)(ad2_4 + 1 * PLANE); SB();
  AMFMA(acc0_3, wf8_3, af8_0); SB();
  wf10_2[3] = __builtin_amdgcn_perm(r10_15, r10_13, 0x05040100); wf10_3[3] = __builtin_amdgcn_perm(r10_15, r10_13, 0x07060302); SB();
  AMFMA(acc0_3, wf9_3, af9_0); SB();
  const u32 r13_14 = *(const u32*)(wb13 + voffA + 1792); SB();
  __builtin_amdgcn_s_waitcnt(0x8978);
  AMFMA(acc1_0, wf8_0, af8_1); SB();
  const u32 r13_15 = *(const u32*)(wb13 + voffA + 1856); wf11_0[0] = __builtin_amdgcn_perm(r11_2, r11_0, 0x05040100); SB();
  AMFMA(acc1_0, wf9_0, af9_1); SB();
  const u32x4 af8_5 = *(LAS u32x4*)(ad2_5 + 0 * PLANE); u32 ad2_6 = (iv4_6[2] << 4) + lb; PIN(ad2_6); SB();
  AMFMA(acc1_1, wf8_1, af8_1); SB();
  wf11_1[0] = __builtin_amdgcn_perm(r11_2, r11_0, 0x07060302); wf11_2[0] = __builtin_amdgcn_perm(r11_3, r11_1, 0x05040100); SB();
  AMFMA(acc1_1, wf9_1, af9_1); SB();
  u32 wo14 = moff2 + 24576; SPIN(wo14); const char* wb14 = W1 + wo14; const u32 r14_0 = *(const u32*)(wb14 + voffA + 0); const u32 ive7 = *(volatile LAS u32*)(jc3 + 25600); SB();
  AMFMA(acc1_2, wf8_2, af8_1); SB();
  const u32 r14_1 = *(const u32*)(wb14 + voffA + 64); SB();
  AMFMA(acc1_2, wf9_2, af9_1); SB();
  const u32x4 af9_5 = *(LAS u32x4*)(ad2_5 + 1 * PLANE); SB();
  AMFMA(acc1_3, wf8_3, af8_1); SB();
  wf11_3[0] = __builtin_amdgcn_perm(r11_3, r11_1, 0x07060302); wf11_0[1] = __builtin_amdgcn_perm(r11_6, r11_4, 0x05040100); SB();
  AMFMA(acc1_3, wf9_3, af9_1); SB();
  const u32 r14_2 = *(const u32*)(wb14 + voffA + 256); SB();
  __builtin_amdgcn_s_waitcnt(0x8978);
  AMFMA(acc2_0, wf8_0, af8_2); SB();
  const u32 r14_3 = *(const u32*)(wb14 + voffA + 320); SB();
  AMFMA(acc2_0, wf9_0, af9_2); SB();
  const u32x4 af8_6 = *(LAS u32x4*)(ad2_6 + 0 * PLANE); const u32 r14_4 = *(const u32*)(wb14 + voffA + 512); SB();
  AMFMA(acc2_1, wf8_1, af8_2); SB();
  wf11_1[1] = __builtin_amdgcn_perm(r11_6, r11_4, 0x07060302); wf11_2[1] = __builtin_amdgcn_perm(r11_7, r11_5, 0x05040100); SB();
  AMFMA(acc2_1, wf9_1, af9_2); SB();
  const u32 r14_5 = *(const u32*)(wb14 + voffA + 576); u32 ad2_7 = (iv4_7[2] << 4) + lb; PIN(ad2_7); SB();
  AMFMA(acc2_2, wf8_2, af8_2); SB();
  const u32 r14_6 = *(const u32*)(wb14 + voffA + 768); SB();
  AMFMA(acc2_2, wf9_2, af9_2); SB();
  const u32x4 af9_6 = *(LAS u32x4*)(ad2_6 + 1 * PLANE); SB();
  AMFMA(acc2_3, wf8_3, af8_2); SB();
  wf11_3[1] = __builtin_amdgcn_perm(r11_7, r11_5, 0x07060302); wf11_0[2] = __builtin_amdgcn_perm(r11_10, r11_8, 0x05040100); SB();
  AMFMA(acc2_3, wf9_3, af9_2); SB();
  const u32 r14_7 = *(const u32*)(wb14 + voffA + 832); SB();
  __builtin_amdgcn_s_waitcnt(0x8879);
  VMFMA(acc3_0, wf8_0, af8_3); SB();
  const u32 r14_8 = *(const u32*)(wb14 + voffA + 1024); wf11_1[2] = __builtin_amdgcn_perm(r11_10, r11_8, 0x07060302); SB();
  VMFMA(acc3_0, wf9_0, af9_3); SB();
  const u32x4 af8_7 = *(LAS u32x4*)(ad2_7 + 0 * PLANE); SB();
  VMFMA(acc3_1, wf8_1, af8_3); SB();
  wf11_2[2] = __builtin_amdgcn_perm(r11_11, r11_9, 0x05040100); wf11_3[2] = __builtin_amdgcn_perm(r11_11, r11_9, 0x07060302); SB();
  VMFMA(acc3_1, wf9_1, af9_3); SB();
  const u32 r14_9 = *(const u32*)(wb14 + voffA + 1088); SB();
  VMFMA(acc3_2, wf8_2, af8_3); SB();
  const u32 r14_10 = *(const u32*)(wb14 + voffA + 1280); SB();
  VMFMA(acc3_2, wf9_2, af9_3); SB();
  const u32x4 af9_7 = *(LAS u32x4*)(ad2_7 + 1 * PLANE); const u32 r14_11 = *(const u32*)(wb14 + voffA + 1344); SB();
  VMFMA(acc3_3, wf8_3, af8_3); SB();
  wf11_0[3] = __builtin_amdgcn_perm(r11_14, r11_12, 0x05040100); wf11_1[3] = __builtin_amdgcn_perm(r11_14, r11_12, 0x07060302); SB();
  VMFMA(acc3_3, wf9_3, af9_3); SB();
  const u32 r14_12 = *(const u32*)(wb14 + voffA + 1536); SB();
  __builtin_amdgcn_s_waitcnt(0x8779);
  AMFMA(acc4_0, wf8_0, af8_4); SB();
  const u32 r14_13 = *(const u32*)(wb14 + voffA + 1600); SB();
  AMFMA(acc4_0, wf9_0, af9_4); SB();
  const u32x4 af10_0 = *(LAS u32x4*)(ad2_0 + 2 * PLANE); SB();
  AMFMA(acc4_1, wf8_1, af8_4); SB();
  wf11_2[3] = __builtin_amdgcn_perm(r11_15, r11_13, 0x05040100); wf11_3[3] = __builtin_amdgcn_perm(r11_15, r11_13, 0x07060302); SB();
  AMFMA(acc4_1, wf9_1, af9_4); SB();
  const u32 r14_14 = *(const u32*)(wb14 + voffA + 1792); SB();
  AMFMA(acc4_2, wf8_2, af8_4); SB();
  const u32 r14_15 = *(const u32*)(wb14 + voffA + 1856); wf12_0[0] = __builtin_amdgcn_perm(r12_2, r12_0, 0x05040100); SB();
  AMFMA(acc4_2, wf9_2, af9_4); SB();
  const u32x4 af11_0 = *(LAS u32x4*)(ad2_0 + 3 * PLANE); SB();
  AMFMA(acc4_3, wf8_3, af8_4); SB();
  wf12_1[0] = __builtin_amdgcn_perm(r12_2, r12_0, 0x07060302); wf12_2[0] = __builtin_amdgcn_perm(r12_3, r12_1, 0x05040100); SB();
  AMFMA(acc4_3, wf9_3, af9_4); SB();
  u32 wo15 = moff3 + 24576; SPIN(wo15); const char* wb15 = W1 + wo15; const u32 r15_0 = *(const u32*)(wb15 + voffA + 0); SB();
  __builtin_amdgcn_s_waitcnt(0x8679);
  AMFMA(acc5_0, wf8_0, af8_5); SB();
  const u32 r15_1 = *(const u32*)(wb15 + voffA + 64); SB();
  AMFMA(acc5_0, wf9_0, af9_5); SB();
  const u32x4 af10_1 = *(LAS u32x4*)(ad2_1 + 2 * PLANE); const u32 r15_2 = *(const u32*)(wb15 + voffA + 256); SB();
  AMFMA(acc5_1, wf8_1, af8_5); SB();
  wf12_3[0] = __builtin_amdgcn_perm(r12_3, r12_1, 0x07060302); wf12_0[1] = __builtin_amdgcn_perm(r12_6, r12_4, 0x05040100); SB();
  AMFMA(acc5_1, wf9_1, af9_5); SB();
  const u32 r15_3 = *(const u32*)(wb15 + voffA + 320); SB();
  __builtin_amdgcn_s_waitcnt(0xc87f); __builtin_amdgcn_s_barrier();
  AMFMA(acc5_2, wf8_2, af8_5); SB();
  const u32 r15_4 = *(const u32*)(wb15 + voffA + 512); SB();
  AMFMA(acc5_2, wf9_2, af9_5); SB();
  const u32x4 af11_1 = *(LAS u32x4*)(ad2_1 + 3 * PLANE); SB();
  AMFMA(acc5_3, wf8_3, af8_5); SB();
  wf12_1[1] = __builtin_amdgcn_perm(r12_6, r12_4, 0x07060302); wf12_2[1] = __builtin_amdgcn_perm(r12_7, r12_5, 0x05040100); SB();
  AMFMA(acc5_3, wf9_3, af9_5); SB();
  const u32 r15_5 = *(const u32*)(wb15 + voffA + 576); SB();
  __builtin_amdgcn_s_waitcnt(0x867a);
  AMFMA(acc6_0, wf8_0, af8_6); SB();
  const u32 r15_6 = *(const u32*)(wb15 + voffA + 768); wf12_3[1] = __builtin_amdgcn_perm(r12_7, r12_5, 0x07060302); SB();
  AMFMA(acc6_0, wf9_0, af9_6); SB();
  const u32x4 af10_2 = *(LAS u32x4*)(ad2_2 + 2 * PLANE); SB();
  AMFMA(acc6_1, wf8_1, af8_6); SB();
  wf12_0[2] = __builtin_amdgcn_perm(r12_10, r12_8, 0x05040100); wf12_1[2] = __builtin_amdgcn_perm(r12_10, r12_8, 0x07060302); SB();
  AMFMA(acc6_1, wf9_1, af9_6); SB();
  const u32 r15_7 = *(const u32*)(wb15 + voffA + 832); SB();
  AMFMA(acc6_2, wf8_2, af8_6); SB();
  const u32 r15_8 = *(const u32*)(wb15 + voffA + 1024); SB();
  AMFMA(acc6_2, wf9_2, af9_6); SB();
  const u32x4 af11_2 = *(LAS u32x4*)(ad2_2 + 3 * PLANE); const u32 r15_9 = *(const u32*)(wb15 + voffA + 1088); SB();
  AMFMA(acc6_3, wf8_3, af8_6); SB();
  wf12_2[2] = __builtin_amdgcn_perm(r12_11, r12_9, 0x05040100); wf12_3[2] = __builtin_amdgcn_perm(r12_11, r12_9, 0x07060302); SB();
  AMFMA(acc6_3, wf9_3, af9_6); SB();
  const u32 r15_10 = *(const u32*)(wb15 + voffA + 1280); SB();
  __builtin_amdgcn_s_waitcnt(0x8678);
  VMFMA(acc7_0, wf8_0, af8_7); SB();
  const u32 r15_11 = *(const u32*)(wb15 + voffA + 1344); SB();
  VMFMA(acc7_0, wf9_0, af9_7); SB();
  const u32x4 af10_3 = *(LAS u32x4*)(ad2_3 + 2 * PLANE); SB();
  VMFMA(acc7_1, wf8_1, af8_7); SB();
  wf12_0[3] = __builtin_amdgcn_perm(r12_14, r12_12, 0x05040100); wf12_1[3] = __builtin_amdgcn_perm(r12_14, r12_12, 0x07060302); SB();
  VMFMA(acc7_1, wf9_1, af9_7); SB();
  const u32 r15_12 = *(const u32*)(wb15 + voffA + 1536); SB();
  VMFMA(acc7_2, wf8_2, af8_7); SB();
  const u32 r15_13 = *(const u32*)(wb15 + voffA + 1600); wf12_2[3] = __builtin_amdgcn_perm(r12_15, r12_13, 0x05040100); SB();
  VMFMA(acc7_2, wf9_2, af9_7); SB();
  const u32x4 af11_3 = *(LAS u32x4*)(ad2_3 + 3 * PLANE); SB();
  VMFMA(acc7_3, wf8_3, af8_7); SB();
  wf12_3[3] = __builtin_amdgcn_perm(r12_15, r12_13, 0x07060302); wf13_0[0] = __builtin_amdgcn_perm(r13_2, r13_0, 0x05040100); SB();
  VMFMA(acc7_3, wf9_3, af9_7); SB();
  const u32 r15_14 = *(const u32*)(wb15 + voffA + 1792); SB();
  __builtin_amdgcn_s_waitcnt(0x8678);
  AMFMA(acc0_0, wf10_0, af10_0); SB();
  const u32 r15_15 = *(const u32*)(wb15 + voffA + 1856); SB();
  AMFMA(acc0_0, wf11_0, af11_0); SB();
  u32 wo16 = moff0 + 0; SPIN(wo16); const char* wb16 = W1 + wo16; const u32 r16_0 = *(const u32*)(wb16 + voffB + 0); SB();
  AMFMA(acc0_1, wf10_1, af10_0); SB();
  wf13_1[0] = __builtin_amdgcn_perm(r13_2, r13_0, 0x07060302); wf13_2[0] = __builtin_amdgcn_perm(r13_3, r13_1, 0x05040100); SB();
  AMFMA(acc0_1, wf11_1, af11_0); SB();
  const u32x4 af10_4 = *(LAS u32x4*)(ad2_4 + 2 * PLANE); const u32 r16_1 = *(const u32*)(wb16 + voffB + 64); SB();
  AMFMA(acc0_2, wf10_2, af10_0); SB();
  const u32 r16_2 = *(const u32*)(wb16 + voffB + 256); SB();
  AMFMA(acc0_2, wf11_2, af11_0); SB();
  const u32x4 af11_4 = *(LAS u32x4*)(ad2_4 + 3 * PLANE); SB();
  AMFMA(acc0_3, wf10_3, af10_0); SB();
  wf13_3[0] = __builtin_amdgcn_perm(r13_3, r13_1, 0x07060302); wf13_0[1] = __builtin_amdgcn_perm(r13_6, r13_4, 0x05040100); SB();
  AMFMA(acc0_3, wf11_3, af11_0); SB();
  const u32 r16_3 = *(const u32*)(wb16 + voffB + 320); SB();
  __builtin_amdgcn_s_waitcnt(0x8679);
  AMFMA(acc1_0, wf10_0, af10_1); SB();
  const u32 r16_4 = *(const u32*)(wb16 + voffB + 512); wf13_1[1] = __builtin_amdgcn_perm(r13_6, r13_4, 0x07060302); SB();
  AMFMA(acc1_0, wf11_0, af11_1); SB();
  const u32x4 af10_5 = *(LAS u32x4*)(ad2_5 + 2 * PLANE); SB();
  AMFMA(acc1_1, wf10_1, af10_1); SB();
  wf13_2[1] = __builtin_amdgcn_perm(r13_7, r13_5, 0x05040100); wf13_3[1] = __builtin_amdgcn_perm(r13_7, r13_5, 0x07060302); SB();
  AMFMA(acc1_1, wf11_1, af11_1); SB();
  const u32 r16_5 = *(const u32*)(wb16 + voffB + 576); SB();
  AMFMA(acc1_2, wf10_2, af10_1); SB();
  const u32 r16_6 = *(const u32*)(wb16 + voffB + 768); SB();
  AMFMA(acc1_2, wf11_2, af11_1); SB();
  const u32x4 af11_5 = *(LAS u32x4*)(ad2_5 + 3 * PLANE); SB();
  AMFMA(acc1_3, wf10_3, af10_1); SB();
  wf13_0[2] = __builtin_amdgcn_perm(r13_10, r13_8, 0x05040100); wf13_1[2] = __builtin_amdgcn_perm(r13_10, r13_8, 0x07060302); SB();
  AMFMA(acc1_3, wf11_3, af11_1); SB();
  const u32 r16_7 = *(const u32*)(wb16 + voffB + 832); SB();
  __builtin_amdgcn_s_waitcnt(0x8679);
  AMFMA(acc2_0, wf10_0, af10_2); SB();
  const u32 r16_8 = *(const u32*)(wb16 + voffB + 1024); SB();
  AMFMA(acc2_0, wf11_0, af11_2); SB();
  const u32 r16_9 = *(const u32*)(wb16 + voffB + 1088); SB();
  AMFMA(acc2_1, wf10_1, af10_2); SB();
  wf13_2[2] = __builtin_amdgcn_perm(r13_11, r13_9, 0x05040100); wf13_3[2] = __builtin_amdgcn_perm(r13_11, r13_9, 0x07060302); SB();
  AMFMA(acc2_1, wf11_1, af11_2); SB();
  const u32x4 af10_6 = *(LAS u32x4*)(ad2_6 + 2 * PLANE); const u32 r16_10 = *(const u32*)(wb16 + voffB + 1280); SB();
  AMFMA(acc2_2, wf10_2, af10_2); SB();
  const u32 r16_11 = *(const u32*)(wb16 + voffB + 1344); SB();
  AMFMA(acc2_2, wf11_2, af11_2); SB();
  const u32x4 af11_6 = *(LAS u32x4*)(ad2_6 + 3 * PLANE); SB();
  AMFMA(acc2_3, wf10_3, af10_2); SB();
  wf13_0[3] = __builtin_amdgcn_perm(r13_14, r13_12, 0x05040100); wf13_1[3] = __builtin_amdgcn_perm(r13_14, r13_12, 0x07060302); SB();
  AMFMA(acc2_3, wf11_3, af11_2); SB();
  const u32 r16_12 = *(const u32*)(wb16 + voffB + 1536); SB();
  __builtin_amdgcn_s_waitcnt(0x8679);
  VMFMA(acc3_0, wf10_0, af10_3); SB();
  const u32 r16_13 = *(const u32*)(wb16 + voffB + 1600); wf13_2[3] = __builtin_amdgcn_perm(r13_15, r13_13, 0x05040100); SB();
  VMFMA(acc3_0, wf11_0, af11_3); SB();
  const u32x4 af10_7 = *(LAS u32x4*)(ad2_7 + 2 * PLANE); SB();
  VMFMA(acc3_1, wf10_1, af10_3); SB();
  wf13_3[3] = __builtin_amdgcn_perm(r13_15, r13_13, 0x07060302); wf14_0[0] = __builtin_amdgcn_perm(r14_2, r14_0, 0x05040100); SB();
  VMFMA(acc3_1, wf11_1, af11_3); SB();
  const u32 r16_14 = *(const u32*)(wb16 + voffB + 1792); SB();
  VMFMA(acc3_2, wf10_2, af10_3); SB();
  const u32 r16_15 = *(const u32*)(wb16 + voffB + 1856); u32 ad3_0 = (iv4_0[3] << 4) + lb; PIN(ad3_0); SB();
  VMFMA(acc3_2, wf11_2, af11_3); SB();
  const u32x4 af11_7 = *(LAS u32x4*)(ad2_7 + 3 * PLANE); u32 wo17 = moff1 + 0; SPIN(wo17); const char* wb17 = W1 + wo17; const u32 r17_0 = *(const u32*)(wb17 + voffB + 0); SB();
  VMFMA(acc3_3, wf10_3, af10_3); SB();
  wf14_1[0] = __builtin_amdgcn_perm(r14_2, r14_0, 0x07060302); wf14_2[0] = __builtin_amdgcn_perm(r14_3, r14_1, 0x05040100); SB();
  VMFMA(acc3_3, wf11_3, af11_3); SB();
  const u32 r17_1 = *(const u32*)(wb17 + voffB + 64); SB();
  __builtin_amdgcn_s_waitcnt(0x867a);
  AMFMA(acc4_0, wf10_0, af10_4); SB();
  const u32 r17_2 = *(const u32*)(wb17 + voffB + 256); SB();
  AMFMA(acc4_0, wf11_0, af11_4); SB();
  u32 ad3_1 = (iv4_1[3] << 4) + lb; PIN(ad3_1); SB();
  AMFMA(acc4_1, wf10_1, af10_4); SB();
  wf14_3[0] = __builtin_amdgcn_perm(r14_3, r14_1, 0x07060302); wf14_0[1] = __builtin_amdgcn_perm(r14_6, r14_4, 0x05040100); SB();
  AMFMA(acc4_1, wf11_1, af11_4); SB();
  const u32x4 af12_0 = *(LAS u32x4*)(ad3_0 + 0 * PLANE); const u32 r17_3 = *(const u32*)(wb17 + voffB + 320); SB();
  AMFMA(acc4_2, wf10_2, af10_4); SB();
  const u32 r17_4 = *(const u32*)(wb17 + voffB + 512); wf14_1[1] = __builtin_amdgcn_perm(r14_6, r14_4, 0x07060302); SB();
  AMFMA(acc4_2, wf11_2, af11_4); SB();
  const u32x4 af13_0 = *(LAS u32x4*)(ad3_0 + 1 * PLANE); SB();
  AMFMA(acc4_3, wf10_3, af10_4); SB();
  wf14_2[1] = __builtin_amdgcn_perm(r14_7, r14_5, 0x05040100); wf14_3[1] = __builtin_amdgcn_perm(r14_7, r14_5, 0x07060302); SB();
  AMFMA(acc4_3, wf11_3, af11_4); SB();
  const u32 r17_5 = *(const u32*)(wb17 + voffB + 576); SB();
  __builtin_amdgcn_s_waitcnt(0x867a);
  AMFMA(acc5_0, wf10_0, af10_5); SB();
  const u32 r17_6 = *(const u32*)(wb17 + voffB + 768); SB();
  AMFMA(acc5_0, wf11_0, af11_5); SB();
  const u32x4 af12_1 = *(LAS u32x4*)(ad3_1 + 0 * PLANE); const u32 r17_7 = *(const u32*)(wb17 + voffB + 832); SB();
  AMFMA(acc5_1, wf10_1, af10_5); SB();
  wf14_0[2] = __builtin_amdgcn_perm(r14_10, r14_8, 0x05040100); wf14_1[2] = __builtin_amdgcn_perm(r14_10, r14_8, 0x07060302); SB();
  AMFMA(acc5_1, wf11_1, af11_5); SB();
  const u32 r17_8 = *(const u32*)(wb17 + voffB + 1024); SB();
  AMFMA(acc5_2, wf10_2, af10_5); SB();
  const u32 r17_9 = *(const u32*)(wb17 + voffB + 1088); SB();
  AMFMA(acc5_2, wf11_2, af11_5); SB();
  const u32x4 af13_1 = *(LAS u32x4*)(ad3_1 + 1 * PLANE); u32 ad3_2 = (iv4_2[3] << 4) + lb; PIN(ad3_2); SB();
  AMFMA(acc5_3, wf10_3, af10_5); SB();
  wf14_2[2] = __builtin_amdgcn_perm(r14_11, r14_9, 0x05040100); wf14_3[2] = __builtin_amdgcn_perm(r14_11, r14_9, 0x07060302); SB();
  AMFMA(acc5_3, wf11_3, af11_5); SB();
  const u32 r17_10 = *(const u32*)(wb17 + voffB + 1280); SB();
  __builtin_amdgcn_s_waitcnt(0x8678);
  AMFMA(acc6_0, wf10_0, af10_6); SB();
  const u32 r17_11 = *(const u32*)(wb17 + voffB + 1344); wf14_0[3] = __builtin_amdgcn_perm(r14_14, r14_12, 0x05040100); SB();
  AMFMA(acc6_0, wf11_0, af11_6); SB();
  u32 ad3_3 = (iv4_3[3] << 4) + lb; PIN(ad3_3); SB();
  AMFMA(acc6_1, wf10_1, af10_6); SB();
  wf14_1[3] = __builtin_amdgcn_perm(r14_14, r14_12, 0x07060302); wf14_2[3] = __builtin_amdgcn_perm(r14_15, r14_13, 0x05040100); SB();
  AMFMA(acc6_1, wf11_1, af11_6); SB();
  const u32x4 af12_2 = *(LAS u32x4*)(ad3_2 + 0 * PLANE); const u32 r17_12 = *(const u32*)(wb17 + voffB + 1536); SB();
  AMFMA(acc6_2, wf10_2, af10_6); SB();
  const u32 r17_13 = *(const u32*)(wb17 + voffB + 1600); SB();
  AMFMA(acc6_2, wf11_2, af11_6); SB();
  const u32x4 af13_2 = *(LAS u32x4*)(ad3_2 + 1 * PLANE); const u32 r17_14 = *(const u32*)(wb17 + voffB + 1792); SB();
  AMFMA(acc6_3, wf10_3, af10_6); SB();
  wf14_3[3] = __builtin_amdgcn_perm(r14_15, r14_13, 0x07060302); wf15_0[0] = __builtin_amdgcn_perm(r15_2, r15_0, 0x05040100); SB();
  AMFMA(acc6_3, wf11_3, af11_6); SB();
  const u32 r17_15 = *(const u32*)(wb17 + voffB + 1856); SB();
  __builtin_amdgcn_s_waitcnt(0x8679);
  VMFMA(acc7_0, wf10_0, af10_7); SB();
  u32 wo18 = moff2 + 0; SPIN(wo18); const char* wb18 = W1 + wo18; const u32 r18_0 = *(const u32*)(wb18 + voffB + 0); SB();
  VMFMA(acc7_0, wf11_0, af11_7); SB();
  const u32x4 af12_3 = *(LAS u32x4*)(ad3_3 + 0 * PLANE); SB();
  VMFMA(acc7_1, wf10_1, af10_7); SB();
  wf15_1[0] = __builtin_amdgcn_perm(r15_2, r15_0, 0x07060302); wf15_2[0] = __builtin_amdgcn_perm(r15_3, r15_1, 0x05040100); SB();
  VMFMA(acc7_1, wf11_1, af11_7); SB();
  const u32 r18_1 = *(const u32*)(wb18 + voffB + 64); SB();
  VMFMA(acc7_2, wf10_2, af10_7); SB();
  const u32 r18_2 = *(const u32*)(wb18 + voffB + 256); wf15_3[0] = __builtin_amdgcn_perm(r15_3, r15_1, 0x07060302); SB();
  VMFMA(acc7_2, wf11_2, af11_7); SB();
  const u32x4 af13_3 = *(LAS u32x4*)(ad3_3 + 1 * PLANE); u32 ad3_4 = (iv4_4[3] << 4) + lb; PIN(ad3_4); SB();
  VMFMA(acc7_3, wf10_3, af10_7); SB();
  wf15_0[1] = __builtin_amdgcn_perm(r15_6, r15_4, 0x05040100); wf15_1[1] = __builtin_amdgcn_perm(r15_6, r15_4, 0x07060302); SB();
  VMFMA(acc7_3, wf11_3, af11_7); SB();
  const u32 r18_3 = *(const u32*)(wb18 + voffB + 320); SB();
  __builtin_amdgcn_s_waitcnt(0x8679);
  AMFMA(acc0_0, wf12_0, af12_0); SB();
  const u32 r18_4 = *(const u32*)(wb18 + voffB + 512); SB();
  AMFMA(acc0_0, wf13_0, af13_0); SB();
  const u32 r18_5 = *(const u32*)(wb18 + voffB + 576); u32 ad3_5 = (iv4_5[3] << 4) + lb; PIN(ad3_5); SB();
  AMFMA(acc0_1, wf12_1, af12_0); SB();
  wf15_2[1] = __builtin_amdgcn_perm(r15_7, r15_5, 0x05040100); wf15_3[1] = __builtin_amdgcn_perm(r15_7, r15_5, 0x07060302); SB();
  AMFMA(acc0_1, wf13_1, af13_0); SB();
  const u32x4 af12_4 = *(LAS u32x4*)(ad3_4 + 0 * PLANE); const u32 r18_6 = *(const u32*)(wb18 + voffB + 768); SB();
  AMFMA(acc0_2, wf12_2, af12_0); SB();
  const u32 r18_7 = *(const u32*)(wb18 + voffB + 832); SB();
  AMFMA(acc0_2, wf13_2, af13_0); SB();
  const u32x4 af13_4 = *(LAS u32x4*)(ad3_4 + 1 * PLANE); SB();
  AMFMA(acc0_3, wf12_3, af12_0); SB();
  wf15_0[2] = __builtin_amdgcn_perm(r15_10, r15_8, 0x05040100); wf15_1[2] = __builtin_amdgcn_perm(r15_10, r15_8, 0x07060302); SB();
  AMFMA(acc0_3, wf13_3, af13_0); SB();
  const u32 r18_8 = *(const u32*)(wb18 + voffB + 1024); SB();
  __builtin_amdgcn_s_waitcnt(0x8679);
  AMFMA(acc1_0, wf12_0, af12_1); SB();
  const u32 r18_9 = *(const u32*)(wb18 + voffB + 1088); wf15_2[2] = __builtin_amdgcn_perm(r15_11, r15_9, 0x05040100); SB();
  AMFMA(acc1_0, wf13_0, af13_1); SB();
  const u32x4 af12_5 = *(LAS u32x4*)(ad3_5 + 0 * PLANE); SB();
  AMFMA(acc1_1, wf12_1, af12_1); SB();
  wf15_3[2] = __builtin_amdgcn_perm(r15_11, r15_9, 0x07060302); wf15_0[3] = __builtin_amdgcn_perm(r15_14, r15_12, 0x05040100); SB();
  AMFMA(acc1_1, wf13_1, af13_1); SB();
  const u32 r18_10 = *(const u32*)(wb18 + voffB + 1280); SB();
  AMFMA(acc1_2, wf12_2, af12_1); SB();
  const u32 r18_11 = *(const u32*)(wb18 + voffB + 1344); SB();
  AMFMA(acc1_2, wf13_2, af13_1); SB();
  const u32x4 af13_5 = *(LAS u32x4*)(ad3_5 + 1 * PLANE); u32 ad3_6 = (iv4_6[3] << 4) + lb; PIN(ad3_6); SB();
  AMFMA(acc1_3, wf12_3, af12_1); SB();
  wf15_1[3] = __builtin_amdgcn_perm(r15_14, r15_12, 0x07060302); wf15_2[3] = __builtin_amdgcn_perm(r15_15, r15_13, 0x05040100); SB();
  AMFMA(acc1_3, wf13_3, af13_1); SB();
  const u32 r18_12 = *(const u32*)(wb18 + voffB + 1536); SB();
  __builtin_amdgcn_s_waitcnt(0x8679);
  AMFMA(acc2_0, wf12_0, af12_2); SB();
  const u32 r18_13 = *(const u32*)(wb18 + voffB + 1600); SB();
  AMFMA(acc2_0, wf13_0, af13_2); SB();
  const u32 r18_14 = *(const u32*)(wb18 + voffB + 1792); u32 ad3_7 = (iv4_7[3] << 4) + lb; PIN(ad3_7); SB();
  AMFMA(acc2_1, wf12_1, af12_2); SB();
  wf15_3[3] = __builtin_amdgcn_perm(r15_15, r15_13, 0x07060302); wf16_0[0] = __builtin_amdgcn_perm(r16_2, r16_0, 0x05040100); SB();
  AMFMA(acc2_1, wf13_1, af13_2); SB();
  const u32x4 af12_6 = *(LAS u32x4*)(ad3_6 + 0 * PLANE); const u32 r18_15 = *(const u32*)(wb18 + voffB + 1856); SB();
  AMFMA(acc2_2, wf12_2, af12_2); SB();
  u32 wo19 = moff3 + 0; SPIN(wo19); const char* wb19 = W1 + wo19; const u32 r19_0 = *(const u32*)(wb19 + voffB + 0); SB();
  AMFMA(acc2_2, wf13_2, af13_2); SB();
  const u32x4 af13_6 = *(LAS u32x4*)(ad3_6 + 1 * PLANE); SB();
  AMFMA(acc2_3, wf12_3, af12_2); SB();
  wf16_1[0] = __builtin_amdgcn_perm(r16_2, r16_0, 0x07060302); wf16_2[0] = __builtin_amdgcn_perm(r16_3, r16_1, 0x05040100); SB();
  AMFMA(acc2_3, wf13_3, af13_2); SB();
  const u32 r19_1 = *(const u32*)(wb19 + voffB + 64); SB();
  __builtin_amdgcn_s_waitcnt(0x867a);
  VMFMA(acc3_0, wf12_0, af12_3); SB();
  const u32 r19_2 = *(const u32*)(wb19 + voffB + 256); wf16_3[0] = __builtin_amdgcn_perm(r16_3, r16_1, 0x07060302); SB();
  VMFMA(acc3_0, wf13_0, af13_3); SB();
  const u32x4 af12_7 = *(LAS u32x4*)(ad3_7 + 0 * PLANE); SB();
  VMFMA(acc3_1, wf12_1, af12_3); SB();
  wf16_0[1] = __builtin_amdgcn_perm(r16_6, r16_4, 0x05040100); wf16_1[1] = __builtin_amdgcn_perm(r16_6, r16_4, 0x07060302); SB();
  VMFMA(acc3_1, wf13_1, af13_3); SB();
  const u32 r19_3 = *(const u32*)(wb19 + voffB + 320); SB();
  VMFMA(acc3_2, wf12_2, af12_3); SB();
  const u32 r19_4 = *(const u32*)(wb19 + voffB + 512); SB();
  VMFMA(acc3_2, wf13_2, af13_3); SB();
  const u32x4 af13_7 = *(LAS u32x4*)(ad3_7 + 1 * PLANE); const u32 r19_5 = *(const u32*)(wb19 + voffB + 576); SB();
  VMFMA(acc3_3, wf12_3, af12_3); SB();
  wf16_2[1] = __builtin_amdgcn_perm(r16_7, r16_5, 0x05040100); wf16_3[1] = __builtin_amdgcn_perm(r16_7, r16_5, 0x07060302); SB();
  VMFMA(acc3_3, wf13_3, af13_3); SB();
  const u32 r19_6 = *(const u32*)(wb19 + voffB + 768); SB();
  __builtin_amdgcn_s_waitcnt(0x8678);
  AMFMA(acc4_0, wf12_0, af12_4); SB();
  const u32 r19_7 = *(const u32*)(wb19 + voffB + 832); SB();
  AMFMA(acc4_0, wf13_0, af13_4); SB();
  AMFMA(acc4_1, wf12_1, af12_4); SB();
  wf16_0[2] = __builtin_amdgcn_perm(r16_10, r16_8, 0x05040100); wf16_1[2] = __builtin_amdgcn_perm(r16_10, r16_8, 0x07060302); SB();
  AMFMA(acc4_1, wf13_1, af13_4); SB();
  const u32x4 af14_0 = *(LAS u32x4*)(ad3_0 + 2 * PLANE); const u32 r19_8 = *(const u32*)(wb19 + voffB + 1024); SB();
  AMFMA(acc4_2, wf12_2, af12_4); SB();
  const u32 r19_9 = *(const u32*)(wb19 + voffB + 1088); wf16_2[2] = __builtin_amdgcn_perm(r16_11, r16_9, 0x05040100); SB();
  AMFMA(acc4_2, wf13_2, af13_4); SB();
  const u32x4 af15_0 = *(LAS u32x4*)(ad3_0 + 3 * PLANE); SB();
  AMFMA(acc4_3, wf12_3, af12_4); SB();
  wf16_3[2] = __builtin_amdgcn_perm(r16_11, r16_9, 0x07060302); wf16_0[3] = __builtin_amdgcn_perm(r16_14, r16_12, 0x05040100); SB();
  AMFMA(acc4_3, wf13_3, af13_4); SB();
  const u32 r19_10 = *(const u32*)(wb19 + voffB + 1280); SB();
  __builtin_amdgcn_s_waitcnt(0x8678);
  AMFMA(acc5_0, wf12_0, af12_5); SB();
  const u32 r19_11 = *(const u32*)(wb19 + voffB + 1344); SB();
  AMFMA(acc5_0, wf13_0, af13_5); SB();
  const u32x4 af14_1 = *(LAS u32x4*)(ad3_1 + 2 * PLANE); const u32 r19_12 = *(const u32*)(wb19 + voffB + 1536); SB();
  AMFMA(acc5_1, wf12_1, af12_5); SB();
  wf16_1[3] = __builtin_amdgcn_perm(r16_14, r16_12, 0x07060302); wf16_2[3] = __builtin_amdgcn_perm(r16_15, r16_13, 0x05040100); SB();
  AMFMA(acc5_1, wf13_1, af13_5); SB();
  const u32 r19_13 = *(const u32*)(wb19 + voffB + 1600); SB();
  AMFMA(acc5_2, wf12_2, af12_5); SB();
  const u32 r19_14 = *(const u32*)(wb19 + voffB + 1792); SB();
  AMFMA(acc5_2, wf13_2, af13_5); SB();
  const u32x4 af15_1 = *(LAS u32x4*)(ad3_1 + 3 * PLANE); SB();
  AMFMA(acc5_3, wf12_3, af12_5); SB();
  wf16_3[3] = __builtin_amdgcn_perm(r16_15, r16_13, 0x07060302); wf17_0[0] = __builtin_amdgcn_perm(r17_2, r17_0, 0x05040100); SB();
  AMFMA(acc5_3, wf13_3, af13_5); SB();
  const u32 r19_15 = *(const u32*)(wb19 + voffB + 1856); SB();
  __builtin_amdgcn_s_waitcnt(0x8679);
  AMFMA(acc6_0, wf12_0, af12_6); SB();
  u32 wo20 = moff0 + 8192; SPIN(wo20); const char* wb20 = W1 + wo20; const u32 r20_0 = *(const u32*)(wb20 + voffB + 0); wf17_1[0] = __builtin_amdgcn_perm(r17_2, r17_0, 0x07060302); SB();
  AMFMA(acc6_0, wf13_0, af13_6); SB();
  AMFMA(acc6_1, wf12_1, af12_6); SB();
  wf17_2[0] = __builtin_amdgcn_perm(r17_3, r17_1, 0x05040100); wf17_3[0] = __builtin_amdgcn_perm(r17_3, r17_1, 0x07060302); SB();
  AMFMA(acc6_1, wf13_1, af13_6); SB();
  const u32x4 af14_2 = *(LAS u32x4*)(ad3_2 + 2 * PLANE); const u32 r20_1 = *(const u32*)(wb20 + voffB + 64); SB();
  AMFMA(acc6_2, wf12_2, af12_6); SB();
  const u32 r20_2 = *(const u32*)(wb20 + voffB + 256); SB();
  AMFMA(acc6_2, wf13_2, af13_6); SB();
  const u32x4 af15_2 = *(LAS u32x4*)(ad3_2 + 3 * PLANE); const u32 r20_3 = *(const u32*)(wb20 + voffB + 320); SB();
  AMFMA(acc6_3, wf12_3, af12_6); SB();
  wf17_0[1] = __builtin_amdgcn_perm(r17_6, r17_4, 0x05040100); wf17_1[1] = __builtin_amdgcn_perm(r17_6, r17_4, 0x07060302); SB();
  AMFMA(acc6_3, wf13_3, af13_6); SB();
  const u32 r20_4 = *(const u32*)(wb20 + voffB + 512); SB();
  __builtin_amdgcn_s_waitcnt(0x8679);
  VMFMA(acc7_0, wf12_0, af12_7); SB();
  const u32 r20_5 = *(const u32*)(wb20 + voffB + 576); SB();
  VMFMA(acc7_0, wf13_0, af13_7); SB();
  const u32x4 af14_3 = *(LAS u32x4*)(ad3_3 + 2 * PLANE); SB();
  VMFMA(acc7_1, wf12_1, af12_7); SB();
  wf17_2[1] = __builtin_amdgcn_perm(r17_7, r17_5, 0x05040100); wf17_3[1] = __builtin_amdgcn_perm(r17_7, r17_5, 0x07060302); SB();
  VMFMA(acc7_1, wf13_1, af13_7); SB();
  const u32 r20_6 = *(const u32*)(wb20 + voffB + 768); SB();
  VMFMA(acc7_2, wf12_2, af12_7); SB();
  const u32 r20_7 = *(const u32*)(wb20 + voffB + 832); wf17_0[2] = __builtin_amdgcn_perm(r17_10, r17_8, 0x05040100); SB();
  VMFMA(acc7_2, wf13_2, af13_7); SB();
  const u32x4 af15_3 = *(LAS u32x4*)(ad3_3 + 3 * PLANE); SB();
  VMFMA(acc7_3, wf12_3, af12_7); SB();
  wf17_1[2] = __builtin_amdgcn_perm(r17_10, r17_8, 0x07060302); wf17_2[2] = __builtin_amdgcn_perm(r17_11, r17_9, 0x05040100); SB();
  VMFMA(acc7_3, wf13_3, af13_7); SB();
  const u32 r20_8 = *(const u32*)(wb20 + voffB + 1024); SB();
  __builtin_amdgcn_s_waitcnt(0x8679);
  AMFMA(acc0_0, wf14_0, af14_0); SB();
  const u32 r20_9 = *(const u32*)(wb20 + voffB + 1088); SB();
  AMFMA(acc0_0, wf15_0, af15_0); SB();
  const u32 r20_10 = *(const u32*)(wb20 + voffB + 1280); SB();
  AMFMA(acc0_1, wf14_1, af14_0); SB();
  wf17_3[2] = __builtin_amdgcn_perm(r17_11, r17_9, 0x07060302); wf17_0[3] = __builtin_amdgcn_perm(r17_14, r17_12, 0x05040100); SB();
  AMFMA(acc0_1, wf15_1, af15_0); SB();
  const u32x4 af14_4 = *(LAS u32x4*)(ad3_4 + 2 * PLANE); const u32 r20_11 = *(const u32*)(wb20 + voffB + 1344); SB();
  AMFMA(acc0_2, wf14_2, af14_0); SB();
  const u32 r20_12 = *(const u32*)(wb20 + voffB + 1536); SB();
  AMFMA(acc0_2, wf15_2, af15_0); SB();
  const u32x4 af15_4 = *(LAS u32x4*)(ad3_4 + 3 * PLANE); SB();
  AMFMA(acc0_3, wf14_3, af14_0); SB();
  wf17_1[3] = __builtin_amdgcn_perm(r17_14, r17_12, 0x07060302); wf17_2[3] = __builtin_amdgcn_perm(r17_15, r17_13, 0x05040100); SB();
  AMFMA(acc0_3, wf15_3, af15_0); SB();
  const u32 r20_13 = *(const u32*)(wb20 + voffB + 1600); SB();
  __builtin_amdgcn_s_waitcnt(0x867a);
  AMFMA(acc1_0, wf14_0, af14_1); SB();
  const u32 r20_14 = *(const u32*)(wb20 + voffB + 1792); wf17_3[3] = __builtin_amdgcn_perm(r17_15, r17_13, 0x07060302); SB();
  AMFMA(acc1_0, wf15_0, af15_1); SB();
  const u32x4 af14_5 = *(LAS u32x4*)(ad3_5 + 2 * PLANE); SB();
  AMFMA(acc1_1, wf14_1, af14_1); SB();
  wf18_0[0] = __builtin_amdgcn_perm(r18_2, r18_0, 0x05040100); wf18_1[0] = __builtin_amdgcn_perm(r18_2, r18_0, 0x07060302); SB();
  AMFMA(acc1_1, wf15_1, af15_1); SB();
  const u32 r20_15 = *(const u32*)(wb20 + voffB + 1856); SB();
  AMFMA(acc1_2, wf14_2, af14_1); SB();
  u32 wo21 = moff1 + 8192; SPIN(wo21); const char* wb21 = W1 + wo21; const u32 r21_0 = *(const u32*)(wb21 + voffB + 0); SB();
  AMFMA(acc1_2, wf15_2, af15_1); SB();
  const u32x4 af15_5 = *(LAS u32x4*)(ad3_5 + 3 * PLANE); SB();
  AMFMA(acc1_3, wf14_3, af14_1); SB();
  wf18_2[0] = __builtin_amdgcn_perm(r18_3, r18_1, 0x05040100); wf18_3[0] = __builtin_amdgcn_perm(r18_3, r18_1, 0x07060302); SB();
  AMFMA(acc1_3, wf15_3, af15_1); SB();
  const u32 r21_1 = *(const u32*)(wb21 + voffB + 64); SB();
  __builtin_amdgcn_s_waitcnt(0x867a);
  AMFMA(acc2_0, wf14_0, af14_2); SB();
  const u32 r21_2 = *(const u32*)(wb21 + voffB + 256); SB();
  AMFMA(acc2_0, wf15_0, af15_2); SB();
  const u32x4 af14_6 = *(LAS u32x4*)(ad3_6 + 2 * PLANE); const u32 r21_3 = *(const u32*)(wb21 + voffB + 320); SB();
  AMFMA(acc2_1, wf14_1, af14_2); SB();
  wf18_0[1] = __builtin_amdgcn_perm(r18_6, r18_4, 0x05040100); wf18_1[1] = __builtin_amdgcn_perm(r18_6, r18_4, 0x07060302); SB();
  AMFMA(acc2_1, wf15_1, af15_2); SB();
  const u32x4 af15_6 = *(LAS u32x4*)(ad3_6 + 3 * PLANE); const u32 r21_4 = *(const u32*)(wb21 + voffB + 512); SB();
  AMFMA(acc2_2, wf14_2, af14_2); SB();
  const u32 r21_5 = *(const u32*)(wb21 + voffB + 576); SB();
  AMFMA(acc2_2, wf15_2, af15_2); SB();
  const u32x4 af14_7 = *(LAS u32x4*)(ad3_7 + 2 * PLANE); SB();
  AMFMA(acc2_3, wf14_3, af14_2); SB();
  wf18_2[1] = __builtin_amdgcn_perm(r18_7, r18_5, 0x05040100); wf18_3[1] = __builtin_amdgcn_perm(r18_7, r18_5, 0x07060302); SB();
  AMFMA(acc2_3, wf15_3, af15_2); SB();
  const u32 r21_6 = *(const u32*)(wb21 + voffB + 768); u32 ad4_0 = (iv2_0[0] << 4) + lb; PIN(ad4_0); SB();
  __builtin_amdgcn_s_waitcnt(0x8778);
  VMFMA(acc3_0, wf14_0, af14_3); SB();
  const u32 r21_7 = *(const u32*)(wb21 + voffB + 832); wf18_0[2] = __builtin_amdgcn_perm(r18_10, r18_8, 0x05040100); SB();
  VMFMA(acc3_0, wf15_0, af15_3); SB();
  const u32x4 af15_7 = *(LAS u32x4*)(ad3_7 + 3 * PLANE); SB();
  VMFMA(acc3_1, wf14_1, af14_3); SB();
  wf18_1[2] = __builtin_amdgcn_perm(r18_10, r18_8, 0x07060302); wf18_2[2] = __builtin_amdgcn_perm(r18_11, r18_9, 0x05040100); SB();
  VMFMA(acc3_1, wf15_1, af15_3); SB();
  const u32 r21_8 = *(const u32*)(wb21 + voffB + 1024); SB();
  VMFMA(acc3_2, wf14_2, af14_3); SB();
  const u32 r21_9 = *(const u32*)(wb21 + voffB + 1088); u32 ad4_1 = (iv2_1[0] << 4) + lb; PIN(ad4_1); SB();
  VMFMA(acc3_2, wf15_2, af15_3); SB();
  const u32x4 af16_0 = *(LAS u32x4*)(ad4_0 + 0 * PLANE); const u32 r21_10 = *(const u32*)(wb21 + voffB + 1280); SB();
  VMFMA(acc3_3, wf14_3, af14_3); SB();
  wf18_3[2] = __builtin_amdgcn_perm(r18_11, r18_9, 0x07060302); wf18_0[3] = __builtin_amdgcn_perm(r18_14, r18_12, 0x05040100); SB();
  VMFMA(acc3_3, wf15_3, af15_3); SB();
  const u32 r21_11 = *(const u32*)(wb21 + voffB + 1344); SB();
  __builtin_amdgcn_s_waitcnt(0x8779);
  AMFMA(acc4_0, wf14_0, af14_4); SB();
  const u32 r21_12 = *(const u32*)(wb21 + voffB + 1536); SB();
  AMFMA(acc4_0, wf15_0, af15_4); SB();
  const u32x4 af17_0 = *(LAS u32x4*)(ad4_0 + 1 * PLANE); u32 ad4_2 = (iv2_2[0] << 4) + lb; PIN(ad4_2); SB();
  AMFMA(acc4_1, wf14_1, af14_4); SB();
  wf18_1[3] = __builtin_amdgcn_perm(r18_14, r18_12, 0x07060302); wf18_2[3] = __builtin_amdgcn_perm(r18_15, r18_13, 0x05040100); SB();
  AMFMA(acc4_1, wf15_1, af15_4); SB();
  const u32x4 af16_1 = *(LAS u32x4*)(ad4_1 + 0 * PLANE); const u32 r21_13 = *(const u32*)(wb21 + voffB + 1600); SB();
  AMFMA(acc4_2, wf14_2, af14_4); SB();
  const u32 r21_14 = *(const u32*)(wb21 + voffB + 1792); wf18_3[3] = __builtin_amdgcn_perm(r18_15, r18_13, 0x07060302); SB();
  AMFMA(acc4_2, wf15_2, af15_4); SB();
  const u32x4 af17_1 = *(LAS u32x4*)(ad4_1 + 1 * PLANE); SB();
  AMFMA(acc4_3, wf14_3, af14_4); SB();
  wf19_0[0] = __builtin_amdgcn_perm(r19_2, r19_0, 0x05040100); wf19_1[0] = __builtin_amdgcn_perm(r19_2, r19_0, 0x07060302); SB();
  AMFMA(acc4_3, wf15_3, af15_4); SB();
  const u32 r21_15 = *(const u32*)(wb21 + voffB + 1856); SB();
  __builtin_amdgcn_s_waitcnt(0x8879);
  AMFMA(acc5_0, wf14_0, af14_5); SB();
  u32 wo22 = moff2 + 8192; SPIN(wo22); const char* wb22 = W1 + wo22; const u32 r22_0 = *(const u32*)(wb22 + voffB + 0); SB();
  AMFMA(acc5_0, wf15_0, af15_5); SB();
  const u32x4 af16_2 = *(LAS u32x4*)(ad4_2 + 0 * PLANE); const u32 r22_1 = *(const u32*)(wb22 + voffB + 64); SB();
  AMFMA(acc5_1, wf14_1, af14_5); SB();
  wf19_2[0] = __builtin_amdgcn_perm(r19_3, r19_1, 0x05040100); wf19_3[0] = __builtin_amdgcn_perm(r19_3, r19_1, 0x07060302); SB();
  AMFMA(acc5_1, wf15_1, af15_5); SB();
  const u32 r22_2 = *(const u32*)(wb22 + voffB + 256); u32 ad4_3 = (iv2_3[0] << 4) + lb; PIN(ad4_3); SB();
  AMFMA(acc5_2, wf14_2, af14_5); SB();
  const u32 r22_3 = *(const u32*)(wb22 + voffB + 320); SB();
  AMFMA(acc5_2, wf15_2, af15_5); SB();
  const u32x4 af17_2 = *(LAS u32x4*)(ad4_2 + 1 * PLANE); SB();
  AMFMA(acc5_3, wf14_3, af14_5); SB();
  wf19_0[1] = __builtin_amdgcn_perm(r19_6, r19_4, 0x05040100); wf19_1[1] = __builtin_amdgcn_perm(r19_6, r19_4, 0x07060302); SB();
  AMFMA(acc5_3, wf15_3, af15_5); SB();
  const u32 r22_4 = *(const u32*)(wb22 + voffB + 512); u32 ad4_4 = (iv2_4[0] << 4) + lb; PIN(ad4_4); SB();
  __builtin_amdgcn_s_waitcnt(0x8879);
  AMFMA(acc6_0, wf14_0, af14_6); SB();
  const u32 r22_5 = *(const u32*)(wb22 + voffB + 576); wf19_2[1] = __builtin_amdgcn_perm(r19_7, r19_5, 0x05040100); SB();
  AMFMA(acc6_0, wf15_0, af15_6); SB();
  const u32x4 af16_3 = *(LAS u32x4*)(ad4_3 + 0 * PLANE); SB();
  AMFMA(acc6_1, wf14_1, af14_6); SB();
  wf19_3[1] = __builtin_amdgcn_perm(r19_7, r19_5, 0x07060302); wf19_0[2] = __builtin_amdgcn_perm(r19_10, r19_8, 0x05040100); SB();
  AMFMA(acc6_1, wf15_1, af15_6); SB();
  const u32x4 af17_3 = *(LAS u32x4*)(ad4_3 + 1 * PLANE); const u32 r22_6 = *(const u32*)(wb22 + voffB + 768); SB();
  AMFMA(acc6_2, wf14_2, af14_6); SB();
  const u32 r22_7 = *(const u32*)(wb22 + voffB + 832); SB();
  AMFMA(acc6_2, wf15_2, af15_6); SB();
  const u32x4 af16_4 = *(LAS u32x4*)(ad4_4 + 0 * PLANE); const u32 r22_8 = *(const u32*)(wb22 + voffB + 1024); SB();
  AMFMA(acc6_3, wf14_3, af14_6); SB();
  wf19_1[2] = __builtin_amdgcn_perm(r19_10, r19_8, 0x07060302); wf19_2[2] = __builtin_amdgcn_perm(r19_11, r19_9, 0x05040100); SB();
  AMFMA(acc6_3, wf15_3, af15_6); SB();
  const u32 r22_9 = *(const u32*)(wb22 + voffB + 1088); u32 ad4_5 = (iv2_5[0] << 4) + lb; PIN(ad4_5); SB();
  __builtin_amdgcn_s_waitcnt(0x897a);
  VMFMA(acc7_0, wf14_0, af14_7); SB();
  const u32 r22_10 = *(const u32*)(wb22 + voffB + 1280); SB();
  VMFMA(acc7_0, wf15_0, af15_7); SB();
  const u32x4 af17_4 = *(LAS u32x4*)(ad4_4 + 1 * PLANE); SB();
  VMFMA(acc7_1, wf14_1, af14_7); SB();
  wf19_3[2] = __builtin_amdgcn_perm(r19_11, r19_9, 0x07060302); wf19_0[3] = __builtin_amdgcn_perm(r19_14, r19_12, 0x05040100); SB();
  VMFMA(acc7_1, wf15_1, af15_7); SB();
  const u32 r22_11 = *(const u32*)(wb22 + voffB + 1344); SB();
  VMFMA(acc7_2, wf14_2, af14_7); SB();
  const u32 r22_12 = *(const u32*)(wb22 + voffB + 1536); wf19_1[3] = __builtin_amdgcn_perm(r19_14, r19_12, 0x07060302); SB();
  VMFMA(acc7_2, wf15_2, af15_7); SB();
  const u32x4 af16_5 = *(LAS u32x4*)(ad4_5 + 0 * PLANE); u32 ad4_6 = (iv2_6[0] << 4) + lb; PIN(ad4_6); SB();
  VMFMA(acc7_3, wf14_3, af14_7); SB();
  wf19_2[3] = __builtin_amdgcn_perm(r19_15, r19_13, 0x05040100); wf19_3[3] = __builtin_amdgcn_perm(r19_15, r19_13, 0x07060302); SB();
  VMFMA(acc7_3, wf15_3, af15_7); SB();
  const u32 r22_13 = *(const u32*)(wb22 + voffB + 1600); SB();
  __builtin_amdgcn_s_waitcnt(0x897a);
  AMFMA(acc0_0, wf16_0, af16_0); SB();
  const u32 r22_14 = *(const u32*)(wb22 + voffB + 1792); u32 ad4_7 = (iv2_7[0] << 4) + lb; PIN(ad4_7); SB();
  AMFMA(acc0_0, wf17_0, af17_0); SB();
  const u32x4 af17_5 = *(LAS u32x4*)(ad4_5 + 1 * PLANE); const u32 r22_15 = *(const u32*)(wb22 + voffB + 1856); SB();
  AMFMA(acc0_1, wf16_1, af16_0); SB();
  wf20_0[0] = __builtin_amdgcn_perm(r20_2, r20_0, 0x05040100); wf20_1[0] = __builtin_amdgcn_perm(r20_2, r20_0, 0x07060302); SB();
  AMFMA(acc0_1, wf17_1, af17_0); SB();
  const u32x4 af16_6 = *(LAS u32x4*)(ad4_6 + 0 * PLANE); u32 wo23 = moff3 + 8192; SPIN(wo23); const char* wb23 = W1 + wo23; const u32 r23_0 = *(const u32*)(wb23 + voffB + 0); SB();
  AMFMA(acc0_2, wf16_2, af16_0); SB();
  const u32 r23_1 = *(const u32*)(wb23 + voffB + 64); SB();
  AMFMA(acc0_2, wf17_2, af17_0); SB();
  const u32x4 af17_6 = *(LAS u32x4*)(ad4_6 + 1 * PLANE); SB();
  AMFMA(acc0_3, wf16_3, af16_0); SB();
  wf20_2[0] = __builtin_amdgcn_perm(r20_3, r20_1, 0x05040100); wf20_3[0] = __builtin_amdgcn_perm(r20_3, r20_1, 0x07060302); SB();
  AMFMA(acc0_3, wf17_3, af17_0); SB();
  const u32 r23_2 = *(const u32*)(wb23 + voffB + 256); SB();
  __builtin_amdgcn_s_waitcnt(0x8a78);
  AMFMA(acc1_0, wf16_0, af16_1); SB();
  const u32 r23_3 = *(const u32*)(wb23 + voffB + 320); wf20_0[1] = __builtin_amdgcn_perm(r20_6, r20_4, 0x05040100); SB();
  AMFMA(acc1_0, wf17_0, af17_1); SB();
  const u32x4 af16_7 = *(LAS u32x4*)(ad4_7 + 0 * PLANE); SB();
  AMFMA(acc1_1, wf16_1, af16_1); SB();
  wf20_1[1] = __builtin_amdgcn_perm(r20_6, r20_4, 0x07060302); wf20_2[1] = __builtin_amdgcn_perm(r20_7, r20_5, 0x05040100); SB();
  AMFMA(acc1_1, wf17_1, af17_1); SB();
  const u32 r23_4 = *(const u32*)(wb23 + voffB + 512); SB();
  AMFMA(acc1_2, wf16_2, af16_1); SB();
  const u32 r23_5 = *(const u32*)(wb23 + voffB + 576); SB();
  AMFMA(acc1_2, wf17_2, af17_1); SB();
  const u32x4 af17_7 = *(LAS u32x4*)(ad4_7 + 1 * PLANE); SB();
  AMFMA(acc1_3, wf16_3, af16_1); SB();
  wf20_3[1] = __builtin_amdgcn_perm(r20_7, r20_5, 0x07060302); wf20_0[2] = __builtin_amdgcn_perm(r20_10, r20_8, 0x05040100); SB();
  AMFMA(acc1_3, wf17_3, af17_1); SB();
  const u32 r23_6 = *(const u32*)(wb23 + voffB + 768); SB();
  __builtin_amdgcn_s_waitcnt(0x8a78);
  AMFMA(acc2_0, wf16_0, af16_2); SB();
  const u32 r23_7 = *(const u32*)(wb23 + voffB + 832); SB();
  AMFMA(acc2_0, wf17_0, af17_2); SB();
  const u32x4 af18_0 = *(LAS u32x4*)(ad4_0 + 2 * PLANE); const u32 r23_8 = *(const u32*)(wb23 + voffB + 1024); SB();
  AMFMA(acc2_1, wf16_1, af16_2); SB();
  wf20_1[2] = __builtin_amdgcn_perm(r20_10, r20_8, 0x07060302); wf20_2[2] = __builtin_amdgcn_perm(r20_11, r20_9, 0x05040100); SB();
  AMFMA(acc2_1, wf17_1, af17_2); SB();
  const u32x4 af19_0 = *(LAS u32x4*)(ad4_0 + 3 * PLANE); const u32 r23_9 = *(const u32*)(wb23 + voffB + 1088); SB();
  AMFMA(acc2_2, wf16_2, af16_2); SB();
  const u32 r23_10 = *(const u32*)(wb23 + voffB + 1280); SB();
  AMFMA(acc2_2, wf17_2, af17_2); SB();
  const u32x4 af18_1 = *(LAS u32x4*)(ad4_1 + 2 * PLANE); SB();
  AMFMA(acc2_3, wf16_3, af16_2); SB();
  wf20_3[2] = __builtin_amdgcn_perm(r20_11, r20_9, 0x07060302); wf20_0[3] = __builtin_amdgcn_perm(r20_14, r20_12, 0x05040100); SB();
  AMFMA(acc2_3, wf17_3, af17_2); SB();
  const u32 r23_11 = *(const u32*)(wb23 + voffB + 1344); SB();
  __builtin_amdgcn_s_waitcnt(0x8b79);
  VMFMA(acc3_0, wf16_0, af16_3); SB();
  const u32 r23_12 = *(const u32*)(wb23 + voffB + 1536); wf20_1[3] = __builtin_amdgcn_perm(r20_14, r20_12, 0x07060302); SB();
  VMFMA(acc3_0, wf17_0, af17_3); SB();
  const u32x4 af19_1 = *(LAS u32x4*)(ad4_1 + 3 * PLANE); SB();
  VMFMA(acc3_1, wf16_1, af16_3); SB();
  wf20_2[3] = __builtin_amdgcn_perm(r20_15, r20_13, 0x05040100); wf20_3[3] = __builtin_amdgcn_perm(r20_15, r20_13, 0x07060302); SB();
  VMFMA(acc3_1, wf17_1, af17_3); SB();
  const u32 r23_13 = *(const u32*)(wb23 + voffB + 1600); SB();
  VMFMA(acc3_2, wf16_2, af16_3); SB();
  const u32 r23_14 = *(const u32*)(wb23 + voffB + 1792); SB();
  VMFMA(acc3_2, wf17_2, af17_3); SB();
  const u32x4 af18_2 = *(LAS u32x4*)(ad4_2 + 2 * PLANE); const u32 r23_15 = *(const u32*)(wb23 + voffB + 1856); SB();
  VMFMA(acc3_3, wf16_3, af16_3); SB();
  wf21_0[0] = __builtin_amdgcn_perm(r21_2, r21_0, 0x05040100); wf21_1[0] = __builtin_amdgcn_perm(r21_2, r21_0, 0x07060302); SB();
  VMFMA(acc3_3, wf17_3, af17_3); SB();
  const char* wb24 = W1; const u32 r24_0 = *(const u32*)(wb24 + voffE + 0); SB();
  __builtin_amdgcn_s_waitcnt(0x8b79);
  AMFMA(acc4_0, wf16_0, af16_4); SB();
  const u32 r24_1 = *(const u32*)(wb24 + voffE + 64); SB();
  AMFMA(acc4_0, wf17_0, af17_4); SB();
  const u32x4 af19_2 = *(LAS u32x4*)(ad4_2 + 3 * PLANE); u32 ad5_0 = (iv2_0[1] << 4) + lb; PIN(ad5_0); SB();
  AMFMA(acc4_1, wf16_1, af16_4); SB();
  wf21_2[0] = __builtin_amdgcn_perm(r21_3, r21_1, 0x05040100); wf21_3[0] = __builtin_amdgcn_perm(r21_3, r21_1, 0x07060302); SB();
  AMFMA(acc4_1, wf17_1, af17_4); SB();
  const u32x4 af18_3 = *(LAS u32x4*)(ad4_3 + 2 * PLANE); const u32 r24_2 = *(const u32*)(wb24 + voffE + 256); SB();
  AMFMA(acc4_2, wf16_2, af16_4); SB();
  const u32 r24_3 = *(const u32*)(wb24 + voffE + 320); wf21_0[1] = __builtin_amdgcn_perm(r21_6, r21_4, 0x05040100); SB();
  AMFMA(acc4_2, wf17_2, af17_4); SB();
  const u32x4 af19_3 = *(LAS u32x4*)(ad4_3 + 3 * PLANE); SB();
  AMFMA(acc4_3, wf16_3, af16_4); SB();
  wf21_1[1] = __builtin_amdgcn_perm(r21_6, r21_4, 0x07060302); wf21_2[1] = __builtin_amdgcn_perm(r21_7, r21_5, 0x05040100); SB();
  AMFMA(acc4_3, wf17_3, af17_4); SB();
  const u32 r24_4 = *(const u32*)(wb24 + voffE + 512); SB();
  __builtin_amdgcn_s_waitcnt(0x8c79);
  AMFMA(acc5_0, wf16_0, af16_5); SB();
  const u32 r24_5 = *(const u32*)(wb24 + voffE + 576); SB();
  AMFMA(acc5_0, wf17_0, af17_5); SB();
  const u32x4 af20_0 = *(LAS u32x4*)(ad5_0 + 0 * PLANE); const u32 r24_6 = *(const u32*)(wb24 + voffE + 768); SB();
  AMFMA(acc5_1, wf16_1, af16_5); SB();
  wf21_3[1] = __builtin_amdgcn_perm(r21_7, r21_5, 0x07060302); wf21_0[2] = __builtin_amdgcn_perm(r21_10, r21_8, 0x05040100); SB();
  AMFMA(acc5_1, wf17_1, af17_5); SB();
  const u32 r24_7 = *(const u32*)(wb24 + voffE + 832); u32 ad5_1 = (iv2_1[1] << 4) + lb; PIN(ad5_1); SB();
  AMFMA(acc5_2, wf16_2, af16_5); SB();
  const u32 r24_8 = *(const u32*)(wb24 + voffE + 1024); SB();
  AMFMA(acc5_2, wf17_2, af17_5); SB();
  const u32x4 af21_0 = *(LAS u32x4*)(ad5_0 + 1 * PLANE); SB();
  AMFMA(acc5_3, wf16_3, af16_5); SB();
  wf21_1[2] = __builtin_amdgcn_perm(r21_10, r21_8, 0x07060302); wf21_2[2] = __builtin_amdgcn_perm(r21_11, r21_9, 0x05040100); SB();
  AMFMA(acc5_3, wf17_3, af17_5); SB();
  const u32 r24_9 = *(const u32*)(wb24 + voffE + 1088); u32 ad5_2 = (iv2_2[1] << 4) + lb; PIN(ad5_2); SB();
  __builtin_amdgcn_s_waitcnt(0x8c7a);
  AMFMA(acc6_0, wf16_0, af16_6); SB();
  const u32 r24_10 = *(const u32*)(wb24 + voffE + 1280); wf21_3[2] = __builtin_amdgcn_perm(r21_11, r21_9, 0x07060302); SB();
  AMFMA(acc6_0, wf17_0, af17_6); SB();
  const u32x4 af20_1 = *(LAS u32x4*)(ad5_1 + 0 * PLANE); SB();
  AMFMA(acc6_1, wf16_1, af16_6); SB();
  wf21_0[3] = __builtin_amdgcn_perm(r21_14, r21_12, 0x05040100); wf21_1[3] = __builtin_amdgcn_perm(r21_14, r21_12, 0x07060302); SB();
  AMFMA(acc6_1, wf17_1, af17_6); SB();
  const u32x4 af21_1 = *(LAS u32x4*)(ad5_1 + 1 * PLANE); const u32 r24_11 = *(const u32*)(wb24 + voffE + 1344); SB();
  AMFMA(acc6_2, wf16_2, af16_6); SB();
  const u32 r24_12 = *(const u32*)(wb24 + voffE + 1536); SB();
  AMFMA(acc6_2, wf17_2, af17_6); SB();
  const u32x4 af20_2 = *(LAS u32x4*)(ad5_2 + 0 * PLANE); const u32 r24_13 = *(const u32*)(wb24 + voffE + 1600); SB();
  AMFMA(acc6_3, wf16_3, af16_6); SB();
  wf21_2[3] = __builtin_amdgcn_perm(r21_15, r21_13, 0x05040100); wf21_3[3] = __builtin_amdgcn_perm(r21_15, r21_13, 0x07060302); SB();
  AMFMA(acc6_3, wf17_3, af17_6); SB();
  const u32 r24_14 = *(const u32*)(wb24 + voffE + 1792); u32 ad5_3 = (iv2_3[1] << 4) + lb; PIN(ad5_3); SB();
  __builtin_amdgcn_s_waitcnt(0x8d78);
  VMFMA(acc7_0, wf16_0, af16_7); SB();
  const u32 r24_15 = *(const u32*)(wb24 + voffE + 1856); SB();
  VMFMA(acc7_0, wf17_0, af17_7); SB();
  const u32x4 af21_2 = *(LAS u32x4*)(ad5_2 + 1 * PLANE); u32 pch = __hip_atomic_load(chal_peer, __ATOMIC_RELAXED, __HIP_MEMORY_SCOPE_AGENT); SB();
  VMFMA(acc7_1, wf16_1, af16_7); SB();
  wf22_0[0] = __builtin_amdgcn_perm(r22_2, r22_0, 0x05040100); wf22_1[0] = __builtin_amdgcn_perm(r22_2, r22_0, 0x07060302); SB();
  VMFMA(acc7_1, wf17_1, af17_7); SB();
  const u32x4 w2v0 = *(const u32x4*)(W2 + hoff); SB();
  VMFMA(acc7_2, wf16_2, af16_7); SB();
  wf22_2[0] = __builtin_amdgcn_perm(r22_3, r22_1, 0x05040100); const u32x4 w2v1 = *(const u32x4*)(W2 + hoff + 64); SB();
  VMFMA(acc7_2, wf17_2, af17_7); SB();
  const u32x4 af20_3 = *(LAS u32x4*)(ad5_3 + 0 * PLANE); const u32x2 xv = *(const u32x2_u*)(xw + xo); SB();
  VMFMA(acc7_3, wf16_3, af16_7); SB();
  wf22_3[0] = __builtin_amdgcn_perm(r22_3, r22_1, 0x07060302); wf22_0[1] = __builtin_amdgcn_perm(r22_6, r22_4, 0x05040100); SB();
  VMFMA(acc7_3, wf17_3, af17_7); SB();
  const u32x2 wv = *(const u32x2_u*)(ww + wo); SB();
  __builtin_amdgcn_s_waitcnt(0x8d75);
  AMFMA(acc0_0, wf18_0, af18_0); SB();
  u32 ae0 = (ive0 << 4) + gplane; PIN(ae0); SB();
  AMFMA(acc0_0, wf19_0, af19_0); SB();
  const u32x4 af21_3 = *(LAS u32x4*)(ad5_3 + 1 * PLANE); wf22_1[1] = __builtin_amdgcn_perm(r22_6, r22_4, 0x07060302); SB();
  AMFMA(acc0_1, wf18_1, af18_0); SB();
  wf22_2[1] = __builtin_amdgcn_perm(r22_7, r22_5, 0x05040100); wf22_3[1] = __builtin_amdgcn_perm(r22_7, r22_5, 0x07060302); SB();
  AMFMA(acc0_1, wf19_1, af19_0); SB();
  const u32x4 af22_0 = *(LAS u32x4*)(ad5_0 + 2 * PLANE); wf22_0[2] = __builtin_amdgcn_perm(r22_10, r22_8, 0x05040100); SB();
  AMFMA(acc0_2, wf18_2, af18_0); SB();
  wf22_1[2] = __builtin_amdgcn_perm(r22_10, r22_8, 0x07060302); wf22_2[2] = __builtin_amdgcn_perm(r22_11, r22_9, 0x05040100); SB();
  AMFMA(acc0_2, wf19_2, af19_0); SB();
  const u32x4 af23_0 = *(LAS u32x4*)(ad5_0 + 3 * PLANE); wf22_3[2] = __builtin_amdgcn_perm(r22_11, r22_9, 0x07060302); SB();
  AMFMA(acc0_3, wf18_3, af18_0); SB();
  wf22_0[3] = __builtin_amdgcn_perm(r22_14, r22_12, 0x05040100); wf22_1[3] = __builtin_amdgcn_perm(r22_14, r22_12, 0x07060302); SB();
  AMFMA(acc0_3, wf19_3, af19_0); SB();
  wf22_2[3] = __builtin_amdgcn_perm(r22_15, r22_13, 0x05040100); wf22_3[3] = __builtin_amdgcn_perm(r22_15, r22_13, 0x07060302); SB();
  __builtin_amdgcn_s_waitcnt(0x8e71);
  AMFMA(acc1_0, wf18_0, af18_1); SB();
  AMFMA(acc1_0, wf19_0, af19_1); SB();
  const u32x4 af24_0 = *(LAS u32x4*)(ae0); SB();
  AMFMA(acc1_1, wf18_1, af18_1); SB();
  AMFMA(acc1_1, wf19_1, af19_1); SB();
  AMFMA(acc1_2, wf18_2, af18_1); SB();
  u32 ae1 = (ive1 << 4) + gplane; PIN(ae1); SB();
  AMFMA(acc1_2, wf19_2, af19_1); SB();
  const u32x4 af22_1 = *(LAS u32x4*)(ad5_1 + 2 * PLANE); wf23_0[0] = __builtin_amdgcn_perm(r23_2, r23_0, 0x05040100); SB();
  AMFMA(acc1_3, wf18_3, af18_1); SB();
  wf23_1[0] = __builtin_amdgcn_perm(r23_2, r23_0, 0x07060302); wf23_2[0] = __builtin_amdgcn_perm(r23_3, r23_1, 0x05040100); SB();
  AMFMA(acc1_3, wf19_3, af19_1); SB();
  wf23_3[0] = __builtin_amdgcn_perm(r23_3, r23_1, 0x07060302); SB();
  __builtin_amdgcn_s_waitcnt(0x4e7d);
  AMFMA(acc2_0, wf18_0, af18_2); SB();
  AMFMA(acc2_0, wf19_0, af19_2); SB();
  const u32x4 af23_1 = *(LAS u32x4*)(ad5_1 + 3 * PLANE); SB();
  AMFMA(acc2_1, wf18_1, af18_2); SB();
  AMFMA(acc2_1, wf19_1, af19_2); SB();
  const u32x4 af24_1 = *(LAS u32x4*)(ae1); SB();
  AMFMA(acc2_2, wf18_2, af18_2); SB();
  AMFMA(acc2_2, wf19_2, af19_2); SB();
  const u32x4 af22_2 = *(LAS u32x4*)(ad5_2 + 2 * PLANE); wf23_0[1] = __builtin_amdgcn_perm(r23_6, r23_4, 0x05040100); SB();
  AMFMA(acc2_3, wf18_3, af18_2); SB();
  wf23_1[1] = __builtin_amdgcn_perm(r23_6, r23_4, 0x07060302); wf23_2[1] = __builtin_amdgcn_perm(r23_7, r23_5, 0x05040100); SB();
  AMFMA(acc2_3, wf19_3, af19_2); SB();
  wf23_3[1] = __builtin_amdgcn_perm(r23_7, r23_5, 0x07060302); u32 ae2 = (ive2 << 4) + gplane; PIN(ae2); SB();
  __builtin_amdgcn_s_waitcnt(0x4f79);
  VMFMA(acc3_0, wf18_0, af18_3); SB();
  VMFMA(acc3_0, wf19_0, af19_3); SB();
  const u32x4 af23_2 = *(LAS u32x4*)(ad5_2 + 3 * PLANE); SB();
  VMFMA(acc3_1, wf18_1, af18_3); SB();
  wf23_0[2] = __builtin_amdgcn_perm(r23_10, r23_8, 0x05040100); wf23_1[2] = __builtin_amdgcn_perm(r23_10, r23_8, 0x07060302); SB();
  VMFMA(acc3_1, wf19_1, af19_3); SB();
  VMFMA(acc3_2, wf18_2, af18_3); SB();
  VMFMA(acc3_2, wf19_2, af19_3); SB();
  const u32x4 af24_2 = *(LAS u32x4*)(ae2); wf23_2[2] = __builtin_amdgcn_perm(r23_11, r23_9, 0x05040100); SB();
  VMFMA(acc3_3, wf18_3, af18_3); SB();
  wf23_3[2] = __builtin_amdgcn_perm(r23_11, r23_9, 0x07060302); SB();
  VMFMA(acc3_3, wf19_3, af19_3); SB();
  u32 ae3 = (ive3 << 4) + gplane; PIN(ae3); SB();
  __builtin_amdgcn_s_waitcnt(0x4f75);
  AMFMA(acc0_0, wf20_0, af20_0); SB();
  AMFMA(acc0_0, wf21_0, af21_0); SB();
  const u32x4 af22_3 = *(LAS u32x4*)(ad5_3 + 2 * PLANE); SB();
  AMFMA(acc0_1, wf20_1, af20_0); SB();
  wf23_0[3] = __builtin_amdgcn_perm(r23_14, r23_12, 0x05040100); wf23_1[3] = __builtin_amdgcn_perm(r23_14, r23_12, 0x07060302); SB();
  AMFMA(acc0_1, wf21_1, af21_0); SB();
  const u32x4 af23_3 = *(LAS u32x4*)(ad5_3 + 3 * PLANE); wf23_2[3] = __builtin_amdgcn_perm(r23_15, r23_13, 0x05040100); SB();
  AMFMA(acc0_2, wf20_2, af20_0); SB();
  wf23_3[3] = __builtin_amdgcn_perm(r23_15, r23_13, 0x07060302); SB();
  AMFMA(acc0_2, wf21_2, af21_0); SB();
  const u32x4 af24_3 = *(LAS u32x4*)(ae3); SB();
  AMFMA(acc0_3, wf20_3, af20_0); SB();
  AMFMA(acc0_3, wf21_3, af21_0); SB();
  __builtin_amdgcn_s_waitcnt(0xf7e);
  AMFMA(acc1_0, wf20_0, af20_1); SB();
  AMFMA(acc1_0, wf21_0, af21_1); SB();
  const u32x4 af18_4 = *(LAS u32x4*)(ad4_4 + 2 * PLANE); wf24_0[0] = __builtin_amdgcn_perm(r24_2, r24_0, 0x05040100); SB();
  AMFMA(acc1_1, wf20_1, af20_1); SB();
  wf24_1[0] = __builtin_amdgcn_perm(r24_2, r24_0, 0x07060302); wf24_2[0] = __builtin_amdgcn_perm(r24_3, r24_1, 0x05040100); SB();
  AMFMA(acc1_1, wf21_1, af21_1); SB();
  wf24_3[0] = __builtin_amdgcn_perm(r24_3, r24_1, 0x07060302); u32 ad5_4 = (iv2_4[1] << 4) + lb; PIN(ad5_4); SB();
  AMFMA(acc1_2, wf20_2, af20_1); SB();
  AMFMA(acc1_2, wf21_2, af21_1); SB();
  const u32x4 af19_4 = *(LAS u32x4*)(ad4_4 + 3 * PLANE); SB();
  AMFMA(acc1_3, wf20_3, af20_1); SB();
  AMFMA(acc1_3, wf21_3, af21_1); SB();
  wf24_0[1] = __builtin_amdgcn_perm(r24_6, r24_4, 0x05040100); wf24_1[1] = __builtin_amdgcn_perm(r24_6, r24_4, 0x07060302); SB();
  __builtin_amdgcn_s_waitcnt(0xf7a);
  AMFMA(acc2_0, wf20_0, af20_2); SB();
  AMFMA(acc2_0, wf21_0, af21_2); SB();
  const u32x4 af20_4 = *(LAS u32x4*)(ad5_4 + 0 * PLANE); wf24_2[1] = __builtin_amdgcn_perm(r24_7, r24_5, 0x05040100); SB();
  AMFMA(acc2_1, wf20_1, af20_2); SB();
  wf24_3[1] = __builtin_amdgcn_perm(r24_7, r24_5, 0x07060302); SB();
  AMFMA(acc2_1, wf21_1, af21_2); SB();
  const u32x4 af21_4 = *(LAS u32x4*)(ad5_4 + 1 * PLANE); SB();
  AMFMA(acc2_2, wf20_2, af20_2); SB();
  AMFMA(acc2_2, wf21_2, af21_2); SB();
  const u32x4 af22_4 = *(LAS u32x4*)(ad5_4 + 2 * PLANE); SB();
  AMFMA(acc2_3, wf20_3, af20_2); SB();
  wf24_0[2] = __builtin_amdgcn_perm(r24_10, r24_8, 0x05040100); wf24_1[2] = __builtin_amdgcn_perm(r24_10, r24_8, 0x07060302); SB();
  AMFMA(acc2_3, wf21_3, af21_2); SB();
  u32 ae4 = (ive4 << 4) + gplane; PIN(ae4); SB();
  __builtin_amdgcn_s_waitcnt(0xf75);
  VMFMA(acc3_0, wf20_0, af20_3); SB();
  VMFMA(acc3_0, wf21_0, af21_3); SB();
  const u32x4 af23_4 = *(LAS u32x4*)(ad5_4 + 3 * PLANE); wf24_2[2] = __builtin_amdgcn_perm(r24_11, r24_9, 0x05040100); SB();
  VMFMA(acc3_1, wf20_1, af20_3); SB();
  wf24_3[2] = __builtin_amdgcn_perm(r24_11, r24_9, 0x07060302); SB();
  VMFMA(acc3_1, wf21_1, af21_3); SB();
  VMFMA(acc3_2, wf20_2, af20_3); SB();
  VMFMA(acc3_2, wf21_2, af21_3); SB();
  const u32x4 af24_4 = *(LAS u32x4*)(ae4); wf24_0[3] = __builtin_amdgcn_perm(r24_14, r24_12, 0x05040100); SB();
  VMFMA(acc3_3, wf20_3, af20_3); SB();
  wf24_1[3] = __builtin_amdgcn_perm(r24_14, r24_12, 0x07060302); wf24_2[3] = __builtin_amdgcn_perm(r24_15, r24_13, 0x05040100); SB();
  VMFMA(acc3_3, wf21_3, af21_3); SB();
  wf24_3[3] = __builtin_amdgcn_perm(r24_15, r24_13, 0x07060302); u32 ad5_5 = (iv2_5[1] << 4) + lb; PIN(ad5_5); SB();
  AMFMA(acc0_0, wf22_0, af22_0); SB();
  AMFMA(acc0_0, wf23_0, af23_0); SB();
  const u32x4 af18_5 = *(LAS u32x4*)(ad4_5 + 2 * PLANE); SB();
  AMFMA(acc0_0, wf24_0, af24_0); SB();
  AMFMA(acc0_1, wf22_1, af22_0); SB();
  const u32x4 af19_5 = *(LAS u32x4*)(ad4_5 + 3 * PLANE); SB();
  AMFMA(acc0_1, wf23_1, af23_0); SB();
  AMFMA(acc0_1, wf24_1, af24_0); SB();
  const u32x4 af20_5 = *(LAS u32x4*)(ad5_5 + 0 * PLANE); SB();
  AMFMA(acc0_2, wf22_2, af22_0); SB();
  AMFMA(acc0_2, wf23_2, af23_0); SB();
  AMFMA(acc0_2, wf24_2, af24_0); SB();
  *(LAS f32x4*)(wa0 + 0) = acc0_0; SB();
  AMFMA(acc0_3, wf22_3, af22_0); SB();
  const u32x4 af21_5 = *(LAS u32x4*)(ad5_5 + 1 * PLANE); SB();
  AMFMA(acc0_3, wf23_3, af23_0); SB();
  AMFMA(acc0_3, wf24_3, af24_0); SB();
  *(LAS f32x4*)(wa0 + 1024) = acc0_1; SB();
  AMFMA(acc1_0, wf22_0, af22_1); SB();
  AMFMA(acc1_0, wf23_0, af23_1); SB();
  AMFMA(acc1_0, wf24_0, af24_1); SB();
  *(LAS f32x4*)(wa0 + 2048) = acc0_2; SB();
  AMFMA(acc1_1, wf22_1, af22_1); SB();
  AMFMA(acc1_1, wf23_1, af23_1); SB();
  AMFMA(acc1_1, wf24_1, af24_1); SB();
  *(LAS f32x4*)(wa0 + 3072) = acc0_3; SB();
  AMFMA(acc1_2, wf22_2, af22_1); SB();
  AMFMA(acc1_2, wf23_2, af23_1); SB();
  AMFMA(acc1_2, wf24_2, af24_1); SB();
  *(LAS f32x4*)(wa1 + 0) = acc1_0; SB();
  AMFMA(acc1_3, wf22_3, af22_1); SB();
  const u32x4 af22_5 = *(LAS u32x4*)(ad5_5 + 2 * PLANE); SB();
  AMFMA(acc1_3, wf23_3, af23_1); SB();
  AMFMA(acc1_3, wf24_3, af24_1); SB();
  *(LAS f32x4*)(wa1 + 1024) = acc1_1; SB();
  AMFMA(acc2_0, wf22_0, af22_2); SB();
  AMFMA(acc2_0, wf23_0, af23_2); SB();
  AMFMA(acc2_0, wf24_0, af24_2); SB();
  *(LAS f32x4*)(wa1 + 2048) = acc1_2; SB();
  AMFMA(acc2_1, wf22_1, af22_2); SB();
  AMFMA(acc2_1, wf23_1, af23_2); SB();
  AMFMA(acc2_1, wf24_1, af24_2); SB();
  *(LAS f32x4*)(wa1 + 3072) = acc1_3; SB();
  AMFMA(acc2_2, wf22_2, af22_2); SB();
  AMFMA(acc2_2, wf23_2, af23_2); SB();
  AMFMA(acc2_2, wf24_2, af24_2); SB();
  *(LAS f32x4*)(wa2 + 0) = acc2_0; SB();
  AMFMA(acc2_3, wf22_3, af22_2); SB();
  const u32x4 af23_5 = *(LAS u32x4*)(ad5_5 + 3 * PLANE); SB();
  AMFMA(acc2_3, wf23_3, af23_2); SB();
  AMFMA(acc2_3, wf24_3, af24_2); SB();
  *(LAS f32x4*)(wa2 + 1024) = acc2_1; SB();
  VMFMA(acc3_0, wf22_0, af22_3); SB();
  VMFMA(acc3_0, wf23_0, af23_3); SB();
  VMFMA(acc3_0, wf24_0, af24_3); SB();
  *(LAS f32x4*)(wa2 + 2048) = acc2_2; SB();
  VMFMA(acc3_1, wf22_1, af22_3); SB();
  u32 ae5 = (ive5 << 4) + gplane; PIN(ae5); SB();
  VMFMA(acc3_1, wf23_1, af23_3); SB();
  VMFMA(acc3_1, wf24_1, af24_3); SB();
  *(LAS f32x4*)(wa2 + 3072) = acc2_3; SB();
  VMFMA(acc3_2, wf22_2, af22_3); SB();
  VMFMA(acc3_2, wf23_2, af23_3); SB();
  VMFMA(acc3_2, wf24_2, af24_3); SB();
  VMFMA(acc3_3, wf22_3, af22_3); SB();
  const u32x4 af24_5 = *(LAS u32x4*)(ae5); SB();
  VMFMA(acc3_3, wf23_3, af23_3); SB();
  VMFMA(acc3_3, wf24_3, af24_3); SB();
  __builtin_amdgcn_s_waitcnt(0xc17f); __builtin_amdgcn_s_barrier();
  AMFMA(acc4_0, wf18_0, af18_4); SB();
  const f32x4 p0_0_0 = *(LAS f32x4*)(ra + 0); SB();
  AMFMA(acc4_0, wf19_0, af19_4); SB();
  const f32x4 p0_1_0 = *(LAS f32x4*)(ra + 4096); SB();
  AMFMA(acc4_0, wf20_0, af20_4); SB();
  const f32x4 p0_2_0 = *(LAS f32x4*)(ra + 8192); SB();
  AMFMA(acc4_0, wf21_0, af21_4); SB();
  const f32x4 p0_0_1 = *(LAS f32x4*)(ra + 1024); SB();
  AMFMA(acc4_0, wf22_0, af22_4); SB();
  const f32x4 p0_1_1 = *(LAS f32x4*)(ra + 5120); SB();
  AMFMA(acc4_0, wf23_0, af23_4); SB();
  const f32x4 p0_2_1 = *(LAS f32x4*)(ra + 9216); SB();
  AMFMA(acc4_0, wf24_0, af24_4); SB();
  const f32x4 p0_0_2 = *(LAS f32x4*)(ra + 2048); SB();
  AMFMA(acc4_1, wf18_1, af18_4); SB();
  const f32x4 p0_1_2 = *(LAS f32x4*)(ra + 6144); SB();
  AMFMA(acc4_1, wf19_1, af19_4); SB();
  const f32x4 p0_2_2 = *(LAS f32x4*)(ra + 10240); SB();
  AMFMA(acc4_1, wf20_1, af20_4); SB();
  const u32x4 af18_6 = *(LAS u32x4*)(ad4_6 + 2 * PLANE); SB();
  AMFMA(acc4_1, wf21_1, af21_4); SB();
  const f32x4 p0_0_3 = *(LAS f32x4*)(ra + 3072); SB();
  AMFMA(acc4_1, wf22_1, af22_4); SB();
  u32 ad5_6 = (iv2_6[1] << 4) + lb; PIN(ad5_6); const f32x4 p0_1_3 = *(LAS f32x4*)(ra + 7168); SB();
  AMFMA(acc4_1, wf23_1, af23_4); SB();
  *(LAS f32x4*)(wa0 + 49152) = acc4_0; SB();
  AMFMA(acc4_1, wf24_1, af24_4); SB();
  const u32x4 af19_6 = *(LAS u32x4*)(ad4_6 + 3 * PLANE); SB();
  AMFMA(acc4_2, wf18_2, af18_4); SB();
  const f32x4 p0_2_3 = *(LAS f32x4*)(ra + 11264); float h3_0_0 = p0_0_0[0] + p0_1_0[0]; PIN(h3_0_0); SB();
  AMFMA(acc4_2, wf19_2, af19_4); SB();
  float h3_0_1 = p0_0_0[1] + p0_1_0[1]; PIN(h3_0_1); float h3_0_2 = p0_0_0[2] + p0_1_0[2]; PIN(h3_0_2); SB();
  AMFMA(acc4_2, wf20_2, af20_4); SB();
  float h3_0_3 = p0_0_0[3] + p0_1_0[3]; PIN(h3_0_3); h3_0_0 += p0_2_0[0]; PIN(h3_0_0); SB();
  AMFMA(acc4_2, wf21_2, af21_4); SB();
  const u32x4 af20_6 = *(LAS u32x4*)(ad5_6 + 0 * PLANE); h3_0_1 += p0_2_0[1]; PIN(h3_0_1); SB();
  AMFMA(acc4_2, wf22_2, af22_4); SB();
  h3_0_2 += p0_2_0[2]; PIN(h3_0_2); h3_0_3 += p0_2_0[3]; PIN(h3_0_3); SB();
  AMFMA(acc4_2, wf23_2, af23_4); SB();
  *(LAS f32x4*)(wa0 + 50176) = acc4_1; SB();
  AMFMA(acc4_2, wf24_2, af24_4); SB();
  float h3_1_0 = p0_0_1[0] + p0_1_1[0]; PIN(h3_1_0); float h3_1_1 = p0_0_1[1] + p0_1_1[1]; PIN(h3_1_1); SB();
  AMFMA(acc4_3, wf18_3, af18_4); SB();
  const u32x4 af21_6 = *(LAS u32x4*)(ad5_6 + 1 * PLANE); float h3_1_2 = p0_0_1[2] + p0_1_1[2]; PIN(h3_1_2); SB();
  AMFMA(acc4_3, wf19_3, af19_4); SB();
  float h3_1_3 = p0_0_1[3] + p0_1_1[3]; PIN(h3_1_3); h3_1_0 += p0_2_1[0]; PIN(h3_1_0); SB();
  AMFMA(acc4_3, wf20_3, af20_4); SB();
  h3_1_1 += p0_2_1[1]; PIN(h3_1_1); h3_1_2 += p0_2_1[2]; PIN(h3_1_2); SB();
  AMFMA(acc4_3, wf21_3, af21_4); SB();
  h3_1_3 += p0_2_1[3]; PIN(h3_1_3); float h3_2_0 = p0_0_2[0] + p0_1_2[0]; PIN(h3_2_0); SB();
  AMFMA(acc4_3, wf22_3, af22_4); SB();
  const u32x4 af22_6 = *(LAS u32x4*)(ad5_6 + 2 * PLANE); float h3_2_1 = p0_0_2[1] + p0_1_2[1]; PIN(h3_2_1); SB();
  AMFMA(acc4_3, wf23_3, af23_4); SB();
  *(LAS f32x4*)(wa0 + 51200) = acc4_2; SB();
  AMFMA(acc4_3, wf24_3, af24_4); SB();
  u32 ae6 = (ive6 << 4) + gplane; PIN(ae6); float h3_2_2 = p0_0_2[2] + p0_1_2[2]; PIN(h3_2_2); SB();
  AMFMA(acc5_0, wf18_0, af18_5); SB();
  float h3_2_3 = p0_0_2[3] + p0_1_2[3]; PIN(h3_2_3); h3_2_0 += p0_2_2[0]; PIN(h3_2_0); SB();
  AMFMA(acc5_0, wf19_0, af19_5); SB();
  const u32x4 af23_6 = *(LAS u32x4*)(ad5_6 + 3 * PLANE); h3_2_1 += p0_2_2[1]; PIN(h3_2_1); SB();
  AMFMA(acc5_0, wf20_0, af20_5); SB();
  h3_2_2 += p0_2_2[2]; PIN(h3_2_2); h3_2_3 += p0_2_2[3]; PIN(h3_2_3); SB();
  AMFMA(acc5_0, wf21_0, af21_5); SB();
  float h3_3_0 = p0_0_3[0] + p0_1_3[0]; PIN(h3_3_0); float h3_3_1 = p0_0_3[1] + p0_1_3[1]; PIN(h3_3_1); SB();
  AMFMA(acc5_0, wf22_0, af22_5); SB();
  float h3_3_2 = p0_0_3[2] + p0_1_3[2]; PIN(h3_3_2); float h3_3_3 = p0_0_3[3] + p0_1_3[3]; PIN(h3_3_3); SB();
  AMFMA(acc5_0, wf23_0, af23_5); SB();
  const u32x4 af24_6 = *(LAS u32x4*)(ae6); h3_3_0 += p0_2_3[0]; PIN(h3_3_0); SB();
  AMFMA(acc5_0, wf24_0, af24_5); SB();
  *(LAS f32x4*)(wa0 + 52224) = acc4_3; SB();
  AMFMA(acc5_1, wf18_1, af18_5); SB();
  h3_3_1 += p0_2_3[1]; PIN(h3_3_1); h3_3_2 += p0_2_3[2]; PIN(h3_3_2); SB();
  AMFMA(acc5_1, wf19_1, af19_5); SB();
  h3_3_3 += p0_2_3[3]; PIN(h3_3_3); h3_0_0 += acc3_0[0]; PIN(h3_0_0); SB();
  AMFMA(acc5_1, wf20_1, af20_5); SB();
  const u32x4 af18_7 = *(LAS u32x4*)(ad4_7 + 2 * PLANE); h3_0_1 += acc3_0[1]; PIN(h3_0_1); SB();
  AMFMA(acc5_1, wf21_1, af21_5); SB();
  h3_0_2 += acc3_0[2]; PIN(h3_0_2); h3_0_3 += acc3_0[3]; PIN(h3_0_3); SB();
  AMFMA(acc5_1, wf22_1, af22_5); SB();
  u32 ad5_7 = (iv2_7[1] << 4) + lb; PIN(ad5_7); h3_1_0 += acc3_1[0]; PIN(h3_1_0); SB();
  AMFMA(acc5_1, wf23_1, af23_5); SB();
  *(LAS f32x4*)(wa1 + 49152) = acc5_0; SB();
  AMFMA(acc5_1, wf24_1, af24_5); SB();
  const u32x4 af19_7 = *(LAS u32x4*)(ad4_7 + 3 * PLANE); h3_1_1 += acc3_1[1]; PIN(h3_1_1); SB();
  AMFMA(acc5_2, wf18_2, af18_5); SB();
  h3_1_2 += acc3_1[2]; PIN(h3_1_2); h3_1_3 += acc3_1[3]; PIN(h3_1_3); SB();
  AMFMA(acc5_2, wf19_2, af19_5); SB();
  u32 c3_1_0 = pack2(h3_0_0, h3_1_0); PIN(c3_1_0); u32 c3_1_1 = pack2(h3_0_1, h3_1_1); PIN(c3_1_1); SB();
  AMFMA(acc5_2, wf20_2, af20_5); SB();
  u32 c3_1_2 = pack2(h3_0_2, h3_1_2); PIN(c3_1_2); u32 c3_1_3 = pack2(h3_0_3, h3_1_3); PIN(c3_1_3); SB();
  AMFMA(acc5_2, wf21_2, af21_5); SB();
  const u32x4 af20_7 = *(LAS u32x4*)(ad5_7 + 0 * PLANE); q3_0[0] = relu2(c3_1_0); SB();
  AMFMA(acc5_2, wf22_2, af22_5); SB();
  q3_0[1] = relu2(c3_1_1); q3_0[2] = relu2(c3_1_2); SB();
  AMFMA(acc5_2, wf23_2, af23_5); SB();
  *(LAS f32x4*)(wa1 + 50176) = acc5_1; SB();
  AMFMA(acc5_2, wf24_2, af24_5); SB();
  q3_0[3] = relu2(c3_1_3); h3_2_0 += acc3_2[0]; PIN(h3_2_0); SB();
  AMFMA(acc5_3, wf18_3, af18_5); SB();
  const u32x4 af21_7 = *(LAS u32x4*)(ad5_7 + 1 * PLANE); h3_2_1 += acc3_2[1]; PIN(h3_2_1); SB();
  AMFMA(acc5_3, wf19_3, af19_5); SB();
  h3_2_2 += acc3_2[2]; PIN(h3_2_2); h3_2_3 += acc3_2[3]; PIN(h3_2_3); SB();
  AMFMA(acc5_3, wf20_3, af20_5); SB();
  h3_3_0 += acc3_3[0]; PIN(h3_3_0); h3_3_1 += acc3_3[1]; PIN(h3_3_1); SB();
  AMFMA(acc5_3, wf21_3, af21_5); SB();
  h3_3_2 += acc3_3[2]; PIN(h3_3_2); h3_3_3 += acc3_3[3]; PIN(h3_3_3); SB();
  AMFMA(acc5_3, wf22_3, af22_5); SB();
  const u32x4 af22_7 = *(LAS u32x4*)(ad5_7 + 2 * PLANE); u32 c3_3_0 = pack2(h3_2_0, h3_3_0); PIN(c3_3_0); SB();
  AMFMA(acc5_3, wf23_3, af23_5); SB();
  *(LAS f32x4*)(wa1 + 51200) = acc5_2; SB();
  AMFMA(acc5_3, wf24_3, af24_5); SB();
  u32 ae7 = (ive7 << 4) + gplane; PIN(ae7); u32 c3_3_1 = pack2(h3_2_1, h3_3_1); PIN(c3_3_1); SB();
  __builtin_amdgcn_s_waitcnt(0x972);
  AMFMA(acc6_0, wf18_0, af18_6); SB();
  u32 c3_3_2 = pack2(h3_2_2, h3_3_2); PIN(c3_3_2); u32 c3_3_3 = pack2(h3_2_3, h3_3_3); PIN(c3_3_3); SB();
  AMFMA(acc6_0, wf19_0, af19_6); SB();
  const u32x4 af23_7 = *(LAS u32x4*)(ad5_7 + 3 * PLANE); q3_1[0] = relu2(c3_3_0); SB();
  AMFMA(acc6_0, wf20_0, af20_6); SB();
  q3_1[1] = relu2(c3_3_1); q3_1[2] = relu2(c3_3_2); SB();
  AMFMA(acc6_0, wf21_0, af21_6); SB();
  q3_1[3] = relu2(c3_3_3); SB();
  AMFMA(acc6_0, wf22_0, af22_6); SB();
  f32x4 dsv = MFMA(w2v0, q3_0, zero4); SB();
  AMFMA(acc6_0, wf23_0, af23_6); SB();
  const u32x4 af24_7 = *(LAS u32x4*)(ae7); SB();
  AMFMA(acc6_0, wf24_0, af24_6); SB();
  *(LAS f32x4*)(wa1 + 52224) = acc5_3; SB();
  AMFMA(acc6_1, wf18_1, af18_6); SB();
  dsv = MFMA(w2v1, q3_1, dsv); SB();
  __builtin_amdgcn_s_waitcnt(0xc70);
  AMFMA(acc6_1, wf19_1, af19_6); SB();
  AMFMA(acc6_1, wf20_1, af20_6); SB();
  AMFMA(acc6_1, wf21_1, af21_6); SB();
  AMFMA(acc6_1, wf22_1, af22_6); SB();
  AMFMA(acc6_1, wf23_1, af23_6); SB();
  *(LAS f32x4*)(wa2 + 49152) = acc6_0; SB();
  AMFMA(acc6_1, wf24_1, af24_6); SB();
  const u32 sendv = __float_as_uint(dsv[0]); pch &= 0xffffu; __hip_atomic_store(outbox, ((u64)((pch << 16) | mych) << 32) | sendv, __ATOMIC_RELAXED, __HIP_MEMORY_SCOPE_AGENT); SB();
  AMFMA(acc6_2, wf18_2, af18_6); SB();
  u32 w0 = wv[0] & wm0; PIN(w0); u32 w1 = wv[1] & wm1; PIN(w1); SB();
  AMFMA(acc6_2, wf19_2, af19_6); SB();
  float xa = bf2f(xv[0]); PIN(xa); float wa_ = bf2f(w0); PIN(wa_); SB();
  __builtin_amdgcn_s_waitcnt(0xcd7f);
  AMFMA(acc6_2, wf20_2, af20_6); SB();
  float xb = __uint_as_float(xv[0] & 0xffff0000u); PIN(xb); float wb_ = __uint_as_float(w0 & 0xffff0000u); PIN(wb_); SB();
  AMFMA(acc6_2, wf21_2, af21_6); SB();
  float ws_ = xa * wa_; PIN(ws_); xa = bf2f(xv[1]); PIN(xa); SB();
  AMFMA(acc6_2, wf22_2, af22_6); SB();
  ws_ = fmaf(xb, wb_, ws_); PIN(ws_); wa_ = bf2f(w1); PIN(wa_); SB();
  AMFMA(acc6_2, wf23_2, af23_6); SB();
  *(LAS f32x4*)(wa2 + 50176) = acc6_1; SB();
  AMFMA(acc6_2, wf24_2, af24_6); SB();
  xb = __uint_as_float(xv[1] & 0xffff0000u); PIN(xb); wb_ = __uint_as_float(w1 & 0xffff0000u); PIN(wb_); SB();
  AMFMA(acc6_3, wf18_3, af18_6); SB();
  ws_ = fmaf(xa, wa_, ws_); PIN(ws_); SB();
  AMFMA(acc6_3, wf19_3, af19_6); SB();
  ws_ = fmaf(xb, wb_, ws_); PIN(ws_); SB();
  AMFMA(acc6_3, wf20_3, af20_6); SB();
  ws_ = sum16(ws_); PIN(ws_); SB();
  __builtin_amdgcn_s_waitcnt(0xc77f);
  AMFMA(acc6_3, wf21_3, af21_6); SB();
  ws_ = sum32(ws_); PIN(ws_); SB();
  AMFMA(acc6_3, wf22_3, af22_6); SB();
  ws_ = rbf(ws_); PIN(ws_); SB();
  AMFMA(acc6_3, wf23_3, af23_6); SB();
  *(LAS f32x4*)(wa2 + 51200) = acc6_2; SB();
  AMFMA(acc6_3, wf24_3, af24_6); SB();
  float wide = rbf(ws_ + bf2f(bwr)); PIN(wide); SB();
  VMFMA(acc7_0, wf18_0, af18_7); SB();
  VMFMA(acc7_0, wf19_0, af19_7); SB();
  VMFMA(acc7_0, wf20_0, af20_7); SB();
  VMFMA(acc7_0, wf21_0, af21_7); SB();
  __builtin_amdgcn_s_waitcnt(0xc47f);
  VMFMA(acc7_0, wf22_0, af22_7); SB();
  u64 got0; POLL(got0); SB();
  VMFMA(acc7_0, wf23_0, af23_7); SB();
  *(LAS f32x4*)(wa2 + 52224) = acc6_3; SB();
  VMFMA(acc7_0, wf24_0, af24_7); SB();
  VMFMA(acc7_1, wf18_1, af18_7); SB();
  VMFMA(acc7_1, wf19_1, af19_7); SB();
  __builtin_amdgcn_s_waitcnt(0xc07f); __builtin_amdgcn_s_barrier();
  VMFMA(acc7_1, wf20_1, af20_7); SB();
  const f32x4 p1_0_0 = *(LAS f32x4*)(ra + 49152); const f32x4 p1_1_0 = *(LAS f32x4*)(ra + 53248); SB();
  VMFMA(acc7_1, wf21_1, af21_7); SB();
  const f32x4 p1_2_0 = *(LAS f32x4*)(ra + 57344); const f32x4 p1_0_1 = *(LAS f32x4*)(ra + 50176); SB();
  VMFMA(acc7_1, wf22_1, af22_7); SB();
  const f32x4 p1_1_1 = *(LAS f32x4*)(ra + 54272); const f32x4 p1_2_1 = *(LAS f32x4*)(ra + 58368); SB();
  __builtin_amdgcn_s_waitcnt(0xcb7f);
  VMFMA(acc7_1, wf23_1, af23_7); SB();
  const f32x4 p1_0_2 = *(LAS f32x4*)(ra + 51200); const f32x4 p1_1_2 = *(LAS f32x4*)(ra + 55296); SB();
  VMFMA(acc7_1, wf24_1, af24_7); SB();
  const f32x4 p1_2_2 = *(LAS f32x4*)(ra + 59392); const f32x4 p1_0_3 = *(LAS f32x4*)(ra + 52224); SB();
  VMFMA(acc7_2, wf18_2, af18_7); SB();
  const f32x4 p1_1_3 = *(LAS f32x4*)(ra + 56320); const f32x4 p1_2_3 = *(LAS f32x4*)(ra + 60416); SB();
  VMFMA(acc7_2, wf19_2, af19_7); SB();
  VMFMA(acc7_2, wf20_2, af20_7); SB();
  VMFMA(acc7_2, wf21_2, af21_7); SB();
  VMFMA(acc7_2, wf22_2, af22_7); SB();
  float h7_0_0 = p1_0_0[0] + p1_1_0[0]; PIN(h7_0_0); float h7_0_1 = p1_0_0[1] + p1_1_0[1]; PIN(h7_0_1); u64 got1; POLL(got1); SB();
  VMFMA(acc7_2, wf23_2, af23_7); SB();
  float h7_0_2 = p1_0_0[2] + p1_1_0[2]; PIN(h7_0_2); float h7_0_3 = p1_0_0[3] + p1_1_0[3]; PIN(h7_0_3); SB();
  VMFMA(acc7_2, wf24_2, af24_7); SB();
  h7_0_0 += p1_2_0[0]; PIN(h7_0_0); h7_0_1 += p1_2_0[1]; PIN(h7_0_1); SB();
  VMFMA(acc7_3, wf18_3, af18_7); SB();
  h7_0_2 += p1_2_0[2]; PIN(h7_0_2); h7_0_3 += p1_2_0[3]; PIN(h7_0_3); SB();
  VMFMA(acc7_3, wf19_3, af19_7); SB();
  float h7_1_0 = p1_0_1[0] + p1_1_1[0]; PIN(h7_1_0); float h7_1_1 = p1_0_1[1] + p1_1_1[1]; PIN(h7_1_1); SB();
  VMFMA(acc7_3, wf20_3, af20_7); SB();
  float h7_1_2 = p1_0_1[2] + p1_1_1[2]; PIN(h7_1_2); float h7_1_3 = p1_0_1[3] + p1_1_1[3]; PIN(h7_1_3); SB();
  VMFMA(acc7_3, wf21_3, af21_7); SB();
  h7_1_0 += p1_2_1[0]; PIN(h7_1_0); h7_1_1 += p1_2_1[1]; PIN(h7_1_1); SB();
  VMFMA(acc7_3, wf22_3, af22_7); SB();
  h7_1_2 += p1_2_1[2]; PIN(h7_1_2); h7_1_3 += p1_2_1[3]; PIN(h7_1_3); SB();
  VMFMA(acc7_3, wf23_3, af23_7); SB();
  float h7_2_0 = p1_0_2[0] + p1_1_2[0]; PIN(h7_2_0); float h7_2_1 = p1_0_2[1] + p1_1_2[1]; PIN(h7_2_1); SB();
  VMFMA(acc7_3, wf24_3, af24_7); SB();
  float h7_2_2 = p1_0_2[2] + p1_1_2[2]; PIN(h7_2_2); float h7_2_3 = p1_0_2[3] + p1_1_2[3]; PIN(h7_2_3); SB();

  SB();
  asm volatile("s_nop 7");
  SB();
  h7_2_0 += p1_2_2[0]; PIN(h7_2_0);
  h7_2_1 += p1_2_2[1]; PIN(h7_2_1);
  h7_2_2 += p1_2_2[2]; PIN(h7_2_2);
  h7_2_3 += p1_2_2[3]; PIN(h7_2_3);
  float h7_3_0 = p1_0_3[0] + p1_1_3[0]; PIN(h7_3_0);
  float h7_3_1 = p1_0_3[1] + p1_1_3[1]; PIN(h7_3_1);
  float h7_3_2 = p1_0_3[2] + p1_1_3[2]; PIN(h7_3_2);
  float h7_3_3 = p1_0_3[3] + p1_1_3[3]; PIN(h7_3_3);
  h7_3_0 += p1_2_3[0]; PIN(h7_3_0);
  h7_3_1 += p1_2_3[1]; PIN(h7_3_1);
  h7_3_2 += p1_2_3[2]; PIN(h7_3_2);
  h7_3_3 += p1_2_3[3]; PIN(h7_3_3);
  h7_0_0 += acc7_0[0]; PIN(h7_0_0);
  h7_0_1 += acc7_0[1]; PIN(h7_0_1);
  h7_0_2 += acc7_0[2]; PIN(h7_0_2);
  h7_0_3 += acc7_0[3]; PIN(h7_0_3);
  h7_1_0 += acc7_1[0]; PIN(h7_1_0);
  h7_1_1 += acc7_1[1]; PIN(h7_1_1);
  h7_1_2 += acc7_1[2]; PIN(h7_1_2);
  h7_1_3 += acc7_1[3]; PIN(h7_1_3);
  u32 c7_1_0 = pack2(h7_0_0, h7_1_0); PIN(c7_1_0);
  u32 c7_1_1 = pack2(h7_0_1, h7_1_1); PIN(c7_1_1);
  u32 c7_1_2 = pack2(h7_0_2, h7_1_2); PIN(c7_1_2);
  u32 c7_1_3 = pack2(h7_0_3, h7_1_3); PIN(c7_1_3);
  q7_0[0] = relu2(c7_1_0);
  q7_0[1] = relu2(c7_1_1);
  q7_0[2] = relu2(c7_1_2);
  q7_0[3] = relu2(c7_1_3);
  h7_2_0 += acc7_2[0]; PIN(h7_2_0);
  h7_2_1 += acc7_2[1]; PIN(h7_2_1);
  h7_2_2 += acc7_2[2]; PIN(h7_2_2);
  h7_2_3 += acc7_2[3]; PIN(h7_2_3);
  h7_3_0 += acc7_3[0]; PIN(h7_3_0);
  h7_3_1 += acc7_3[1]; PIN(h7_3_1);
  h7_3_2 += acc7_3[2]; PIN(h7_3_2);
  h7_3_3 += acc7_3[3]; PIN(h7_3_3);
  u32 c7_3_0 = pack2(h7_2_0, h7_3_0); PIN(c7_3_0);
  u32 c7_3_1 = pack2(h7_2_1, h7_3_1); PIN(c7_3_1);
  u32 c7_3_2 = pack2(h7_2_2, h7_3_2); PIN(c7_3_2);
  u32 c7_3_3 = pack2(h7_2_3, h7_3_3); PIN(c7_3_3);
  q7_1[0] = relu2(c7_3_0);
  q7_1[1] = relu2(c7_3_1);
  q7_1[2] = relu2(c7_3_2);
  q7_1[3] = relu2(c7_3_3);
  f32x4 dk = MFMA(w2v0, q7_0, zero4);
  dk = MFMA(w2v1, q7_1, dk);
  const float b2f = bf2f(b2r);
#define SPINWAIT() \
  _Pragma("nounroll") for (int spin = 0; spin < (1 << 22); ++spin) { \
    if (__all((u32)(got >> 48) == mych)) break; \
    u32 pnew; \
    asm volatile("global_load_dwordx2 %0, %2, %3 sc1\n\tglobal_load_dword %1, %4, %5 offset:128 sc1\n\ts_waitcnt vmcnt(0)" \
                 : "=&v"(got), "=&v"(pnew) : "v"(ibo), "s"(ibase), "v"(cho), "s"(pbase) : "memory"); \
    pnew &= 0xffffu; \
    if (pnew != pch) {        \
      pch = pnew; \
      const u64 gr = ((u64)((pch << 16) | mych) << 32) | sendv; \
      asm volatile("global_store_dwordx2 %0, %1, %2 sc1\n\ts_waitcnt vmcnt(0)\n.Lwd_end2:" :: "v"(ibo), "v"(gr), "s"(pbase) : "memory"); \
    } \
  }
  asm volatile("s_waitcnt vmcnt(1)" : "+v"(got0));
  u64 got = got0;
  if (__builtin_expect(!__all((u32)(got >> 48) == mych), 0)) {
    asm volatile("s_waitcnt vmcnt(0)" : "+v"(got1)); got = got1;
    SPINWAIT();
  }
  const float deep = rbf(rbf(dk[0] + __uint_as_float((u32)got)) + b2f);
  const float z = rbf(wide + deep);
  *(__hip_bfloat16*)((char*)out + oo) = __float2bfloat16(__builtin_amdgcn_rcpf(1.f + __expf(-z)));
  {
    const u32 ptrue = (u32)(got >> 32) & 0xffffu;
    if (__builtin_expect(ptrue != pch, 0)) __hip_atomic_store(outbox, ((u64)((ptrue << 16) | mych) << 32) | sendv, __ATOMIC_RELAXED, __HIP_MEMORY_SCOPE_AGENT);
  }
  asm volatile("s_waitcnt vmcnt(0)" :: "v"(got0), "v"(got1));
  asm volatile(".Lwd_end:");
}

extern "C" void kernel_launch(void* const* d_in, const int* in_sizes, int n_in, void* d_out, int out_size, void* d_ws,
                              size_t ws_size, hipStream_t stream) {
  (void)hipFuncSetAttribute((const void*)wd_main, hipFuncAttributeMaxDynamicSharedMemorySize, LDS_BYTES);
  wd_main<<<256, 256, LDS_BYTES, stream>>>((const int*)d_in[0], (const char*)d_in[1], (const char*)d_in[5], (char*)d_ws,
                                           (const char*)d_in[2], (const char*)d_in[3], (const char*)d_in[6], (const char*)d_in[7],
                                           (const unsigned short*)d_in[4], (const unsigned short*)d_in[8], (__hip_bfloat16*)d_out
  );
}
```
